```python
import torch

def _softmax(x, axis=-1):
    return torch.softmax(x, dim=axis)
def _gelu(x):
    return torch.nn.functional.gelu(x, approximate="tanh")
def _silu(x):
    return torch.nn.functional.silu(x)
def _sigmoid(x):
    return torch.sigmoid(x)
def _layernorm(x, axis=-1, eps=1e-5):
    m = x.mean(dim=axis, keepdim=True)
    v = x.var(dim=axis, keepdim=True, unbiased=False)
    return (x - m) / torch.sqrt(v + eps)
def _scatter_add(tgt, idx, src, axis=0):
    return tgt.clone().index_add_(axis, idx, src)
def _segment_sum(src, seg, n):
    out = torch.zeros((n,) + src.shape[1:], dtype=src.dtype)
    return out.index_add_(0, seg, src)
def _pad(x, pw):
    flat = tuple(v for pair in reversed(pw) for v in pair)
    return torch.nn.functional.pad(x, flat)

kernel_name = 'sequential_preLN__ssm_conv_s4__glu_gated_swiglu__prompt'

def setup_inputs():

    _w = (lambda a: (lambda t: t.to(torch.bfloat16) if t.is_floating_point() else t)(torch.as_tensor(a)))
    def _A_setup():
        B, L, D, N = 1, 2048, 576, 128
        A_re = -(lambda _x: torch.exp(torch.as_tensor(_x)))((lambda *s: torch.randn(*s, dtype=torch.bfloat16) * 6.4)(D, N))
        A_im = torch.pi * (lambda *s: torch.randn(*s, dtype=torch.bfloat16) * 0.05)(D, N)
        B_re = (lambda *s: torch.randn(*s, dtype=torch.bfloat16) * 0.05)(D, N)
        B_im = (lambda *s: torch.randn(*s, dtype=torch.bfloat16) * 0.05)(D, N)
        C_re = (lambda *s: torch.randn(*s, dtype=torch.bfloat16) * 0.05)(D, N)
        C_im = (lambda *s: torch.randn(*s, dtype=torch.bfloat16) * 0.05)(D, N)
        log_dt = (lambda *s: torch.randn(*s, dtype=torch.bfloat16) * 0.05)(D) * 0.5 - 4.0
        Dskip = (lambda *s: torch.randn(*s, dtype=torch.bfloat16) * 0.05)(D)
        return {
            "u": _w((lambda *s: torch.randn(*s, dtype=torch.bfloat16) * 0.05)(B, L, D)),
            "A_re": _w(A_re),
            "A_im": _w(A_im),
            "B_re": _w(B_re),
            "B_im": _w(B_im),
            "C_re": _w(C_re),
            "C_im": _w(C_im),
            "log_dt": _w(log_dt),
            "Dskip": _w(Dskip),
        }
    _A_in = _A_setup()

    def _B_setup():
        B, S, D, F = 1, 2048, 576, 4608
        return {
            "x": _w((lambda *s: torch.randn(*s, dtype=torch.bfloat16) * 0.05)(B, S, D)),
            "w1": _w((lambda *s: torch.randn(*s, dtype=torch.bfloat16) * 0.4)(D, F) * (2.0 / D) ** 0.5),
            "w3": _w((lambda *s: torch.randn(*s, dtype=torch.bfloat16) * 0.4)(D, F) * (2.0 / D) ** 0.5),
            "w2": _w((lambda *s: torch.randn(*s, dtype=torch.bfloat16) * 0.4)(F, D) * (2.0 / F) ** 0.5),
        }
    _B_in = _B_setup()

    return {
        "A_u": _A_in["u"],
        "A_A_re": _A_in["A_re"],
        "A_A_im": _A_in["A_im"],
        "A_B_re": _A_in["B_re"],
        "A_B_im": _A_in["B_im"],
        "A_C_re": _A_in["C_re"],
        "A_C_im": _A_in["C_im"],
        "A_log_dt": _A_in["log_dt"],
        "A_Dskip": _A_in["Dskip"],
        "B_x": _B_in["x"],
        "B_w1": _B_in["w1"],
        "B_w3": _B_in["w3"],
        "B_w2": _B_in["w2"],
    }

def reference(A_u, A_A_re, A_A_im, A_B_re, A_B_im, A_C_re, A_C_im, A_log_dt, A_Dskip, B_x, B_w1, B_w3, B_w2):
    def _A_ref(u, A_re, A_im, B_re, B_im, C_re, C_im, log_dt, Dskip):
        Bsz, L, D = u.shape
        dt = torch.exp(log_dt)[:, None]
        A = A_re + 1j * A_im
        Bc = B_re + 1j * B_im
        Cc = C_re + 1j * C_im
        logdA = dt * A
        dB = dt * Bc
        l = (lambda x, dt: x.to(dt))(torch.arange(L), A_re.dtype)
        P = torch.exp(logdA[:, :, None] * l[None, None, :])
        K = torch.einsum("dn,dnl,dn->dl", Cc, P, dB).real
        L2 = 2 * L
        Kp = _pad(K, ((0, 0), (0, L)))
        up = (lambda x, *d: x.permute(*d))(u, 0, 2, 1)
        up = _pad(up, ((0, 0), (0, 0), (0, L)))
        Kf = (lambda x, axes=(-2,-1): torch.fft.rfft2(x.float(), dim=axes))(Kp[:, None, :], axes=(-2, -1))
        Uf = (lambda x, axes=(-2,-1): torch.fft.rfft2(x.float(), dim=axes))(up[:, :, None, :], axes=(-2, -1))
        Yf = Uf * Kf[None, :, :, :]
        y = (lambda x, s, axes=(-2,-1): torch.fft.irfft2(x, s=s, dim=axes).to(torch.bfloat16))(Yf, (1, L2), axes=(-2, -1))[:, :, 0, :L]
        y = (lambda x, *d: x.permute(*d))(y, 0, 2, 1) + u * Dskip[None, None, :]
        return y

    def _B_ref(x, w1, w3, w2):
        gate = torch.einsum("bsd,df->bsf", x, w1)
        up = torch.einsum("bsd,df->bsf", x, w3)
        h = _silu(gate) * up
        out = torch.einsum("bsf,fd->bsd", h, w2)
        return out

    x = A_u
    h = x + _A_ref(_layernorm(x, axis=-1), A_A_re, A_A_im, A_B_re, A_B_im, A_C_re, A_C_im, A_log_dt, A_Dskip)
    h2 = h + _B_ref(_layernorm(h, axis=-1), B_w1, B_w3, B_w2)
    return h2
```

```cpp
#include <hip/hip_runtime.h>
#include <hip/hip_bf16.h>
#include <stdint.h>
#define MIMIC 1
#define LROUND 1
typedef unsigned short u16;
__device__ __forceinline__ float b2f(u16 v){ return __uint_as_float(((unsigned)v)<<16); }
__device__ __forceinline__ u16 f2b(float f){ __hip_bfloat16 b = __float2bfloat16(f); return *reinterpret_cast<u16*>(&b); }
__device__ __forceinline__ float rb(float f){
  return b2f(f2b(f));
}
constexpr int L=2048, D=576, N=128, F=4608;

__global__ void k_ln(const u16* x, u16* o){
  int r=blockIdx.x; int t=threadIdx.x;
  float v[9]; float s=0;
  for(int i=0;i<9;i++){ v[i]=b2f(x[r*D+t+64*i]); s+=v[i]; }
  for(int o2=32;o2>0;o2>>=1) s+=__shfl_xor(s,o2);
  float m=s/D; float q=0;
  for(int i=0;i<9;i++){ float d=v[i]-m; q+=d*d; }
  for(int o2=32;o2>0;o2>>=1) q+=__shfl_xor(q,o2);
  float var=q/D;
  float mb=rb(m); float sd=rb(sqrtf(rb(rb(var)+1e-5f)));
  for(int i=0;i<9;i++){ o[r*D+t+64*i]=f2b(rb(v[i]-mb)/sd); }
}
__global__ void k_kgen(const u16* Are,const u16* Aim,const u16* Bre,const u16* Bim,const u16* Cre,const u16* Cim,const u16* ldt,float* K){
  int d=blockIdx.x;
  __shared__ float a[N],b[N],cr[N],ci[N];
  float dt=rb(expf(b2f(ldt[d])));
  if(threadIdx.x<N){ int n=threadIdx.x; int i=d*N+n;
    a[n]=dt*b2f(Are[i]); b[n]=dt*b2f(Aim[i]);
    float br=dt*b2f(Bre[i]), bi=dt*b2f(Bim[i]); float c0=b2f(Cre[i]), c1=b2f(Cim[i]);
    cr[n]=c0*br-c1*bi; ci[n]=c0*bi+c1*br; }
  __syncthreads();
  for(int l=threadIdx.x;l<L;l+=blockDim.x){
    float lf=b2f(f2b((float)l));
    float s=0;
    for(int n=0;n<N;n++){ float e=expf(a[n]*lf); float sn,cs; sincosf(b[n]*lf,&sn,&cs); s+=e*(cr[n]*cs-ci[n]*sn); }
    K[d*L+l]=s;
  }
}
__global__ void k_conv(const u16* u,const float* K,const u16* x,const u16* Dsk,u16* h){
  int d=blockIdx.x; __shared__ float su[L], sk[L];
  for(int t=threadIdx.x;t<L;t+=blockDim.x){ su[t]=b2f(u[t*D+d]); sk[t]=K[d*L+t]; }
  __syncthreads();
  float ds=b2f(Dsk[d]);
  for(int t=threadIdx.x;t<L;t+=blockDim.x){
    float s=0; for(int l=0;l<=t;l++) s+=sk[l]*su[t-l];
    float y=rb(rb(s)+rb(su[t]*ds));
    h[t*D+d]=f2b(b2f(x[t*D+d])+y);
  }
}
__global__ void k_g1(const u16* a,const u16* w1,const u16* w3,u16* hf){
  int r0=blockIdx.y*16; int c=blockIdx.x*256+threadIdx.x;
  __shared__ float sa[16][D];
  for(int i=threadIdx.x;i<16*D;i+=256) sa[i/D][i%D]=b2f(a[(r0+i/D)*D+i%D]);
  __syncthreads();
  float g[16],up[16]; for(int i=0;i<16;i++){g[i]=0;up[i]=0;}
  for(int k=0;k<D;k++){ float x1=b2f(w1[k*F+c]), x3=b2f(w3[k*F+c]);
    for(int i=0;i<16;i++){ g[i]+=sa[i][k]*x1; up[i]+=sa[i][k]*x3; } }
  for(int i=0;i<16;i++){ float gg=rb(g[i]), uu=rb(up[i]); float s=rb(gg/(1.f+expf(-gg))); hf[(r0+i)*F+c]=f2b(s*uu); }
}
__global__ void k_g2(const u16* hf,const u16* w2,const u16* h,u16* out){
  int r0=blockIdx.y*8; int c=blockIdx.x*64+threadIdx.x;
  __shared__ float sa[8][512];
  float acc[8]; for(int i=0;i<8;i++)acc[i]=0;
  for(int k0=0;k0<F;k0+=512){
    __syncthreads();
    for(int i=threadIdx.x;i<8*512;i+=64) sa[i/512][i%512]=b2f(hf[(r0+i/512)*F+k0+i%512]);
    __syncthreads();
    for(int k=0;k<512;k++){ float w=b2f(w2[(k0+k)*D+c]); for(int i=0;i<8;i++) acc[i]+=sa[i][k]*w; }
  }
  for(int i=0;i<8;i++) out[(r0+i)*D+c]=f2b(b2f(h[(r0+i)*D+c])+rb(acc[i]));
}
extern "C" void kernel_launch(void* const* d_in, const int* in_sizes, int n_in,
                              void* d_out, int out_size, void* d_ws, size_t ws_size,
                              hipStream_t stream) {
  const u16* x=(const u16*)d_in[0];
  char* ws=(char*)d_ws;
  u16* u=(u16*)ws; u16* h=(u16*)(ws+4*1024*1024); u16* a=(u16*)(ws+8*1024*1024);
  float* K=(float*)(ws+12*1024*1024); u16* hf=(u16*)(ws+32*1024*1024);
  k_ln<<<L,64,0,stream>>>(x,u);
  k_kgen<<<D,256,0,stream>>>((const u16*)d_in[1],(const u16*)d_in[2],(const u16*)d_in[3],(const u16*)d_in[4],(const u16*)d_in[5],(const u16*)d_in[6],(const u16*)d_in[7],K);
  k_conv<<<D,256,0,stream>>>(u,K,x,(const u16*)d_in[8],h);
  k_ln<<<L,64,0,stream>>>(h,a);
  k_g1<<<dim3(F/256,L/16),256,0,stream>>>(a,(const u16*)d_in[10],(const u16*)d_in[11],hf);
  k_g2<<<dim3(D/64,L/8),64,0,stream>>>(hf,(const u16*)d_in[12],h,(u16*)d_out);
}
```

```cpp
#include <hip/hip_runtime.h>
#include <stdint.h>

typedef unsigned short u16;
typedef unsigned int u32;
typedef unsigned long long u64;
typedef __bf16 bf2 __attribute__((ext_vector_type(2)));
typedef __bf16 bf8 __attribute__((ext_vector_type(8)));
typedef short s4 __attribute__((ext_vector_type(4)));
typedef u32 u32x2 __attribute__((ext_vector_type(2)));
typedef u32 u32x4 __attribute__((ext_vector_type(4)));
typedef float f32x2 __attribute__((ext_vector_type(2)));
typedef float f32x4 __attribute__((ext_vector_type(4)));
typedef float f32x16 __attribute__((ext_vector_type(16)));
#define LDS_AS __attribute__((address_space(3)))
#define DEVI __device__ __forceinline__

constexpr int L = 2048, D = 576, NM = 128, F = 4608;

constexpr size_t WS_UT   = 0;
constexpr size_t WS_XT   = WS_UT + (size_t)D * L * 2;
constexpr size_t WS_HT   = WS_XT + (size_t)D * L * 2;
constexpr int    KREV_PITCH = 4352;
constexpr size_t WS_KREV = WS_HT + (size_t)D * L * 2;
constexpr size_t WS_WT   = WS_KREV + (size_t)D * KREV_PITCH;
constexpr size_t WT_BYTES = (size_t)288 * 36 * 1024;
constexpr size_t WS_W2T  = WS_WT + WT_BYTES + 65536;
constexpr size_t W2T_BYTES = (size_t)4 * 144 * 9 * 1024;
constexpr size_t WS_CSP  = WS_W2T + W2T_BYTES + 65536;
constexpr size_t WS_CS   = WS_CSP + (size_t)4 * 288 * 32 * 4;
constexpr size_t WS_HFF  = WS_CS + (size_t)32 * 512 * 4;
constexpr size_t HFF_BYTES = (size_t)128 * 144 * 1024;
constexpr size_t WS_PART = WS_HFF + HFF_BYTES;
constexpr size_t PART_BYTES = (size_t)64 * 4 * 4 * 18432;
constexpr size_t WS_FLAG = WS_PART + PART_BYTES;
constexpr size_t FLAG_BYTES = (size_t)256 * 128;

DEVI float bf2f(u32 v) { return __uint_as_float(v << 16); }
DEVI float bflo(u32 v) { return __uint_as_float(v << 16); }
DEVI float bfhi(u32 v) { return __uint_as_float(v & 0xffff0000u); }
DEVI u32 pk2(float lo, float hi) {
  bf2 r = {(__bf16)lo, (__bf16)hi};
  return __builtin_bit_cast(u32, r);
}
DEVI float rbf(float x) { return __uint_as_float(((u32)__builtin_bit_cast(u16, (__bf16)x)) << 16); }
DEVI void glds16(const void* g, LDS_AS void* l) {
  __builtin_amdgcn_global_load_lds((const __attribute__((address_space(1))) void*)g, l, 16, 0, 0);
}
DEVI void dma16(const char* sbase, u32 voff, u32 lds) {
  asm volatile("s_mov_b32 m0, %2\n\ts_nop 0\n\tglobal_load_lds_dwordx4 %0, %1" ::"v"(voff), "s"(sbase), "s"(lds) : "memory");
}
DEVI void dma12(const char* sbase, u32 voff, u32 lds) {
  asm volatile("s_mov_b32 m0, %2\n\ts_nop 0\n\tglobal_load_lds_dwordx3 %0, %1" ::"v"(voff), "s"(sbase), "s"(lds) : "memory");
}
DEVI void dma4(const char* sbase, u32 voff, u32 lds) {
  asm volatile("s_mov_b32 m0, %2\n\ts_nop 0\n\tglobal_load_lds_dword %0, %1" ::"v"(voff), "s"(sbase), "s"(lds) : "memory");
}
#define OPAQUE(x) asm volatile("" : "+v"(x))
#define PIN() __builtin_amdgcn_sched_barrier(0)
DEVI s4 tr_read(LDS_AS void* p) { return __builtin_amdgcn_ds_read_tr16_b64_v4i16((LDS_AS s4*)p); }
DEVI u32x4 tr_read2(LDS_AS char* p0, LDS_AS char* p1) {
  u32x2 a = __builtin_bit_cast(u32x2, tr_read(p0)), b = __builtin_bit_cast(u32x2, tr_read(p1));
  u32x4 r = {a.x, a.y, b.x, b.y};
  return r;
}
DEVI void st16_wt(__amdgpu_buffer_rsrc_t rs, u32 off, u32x4 v) { __builtin_amdgcn_raw_buffer_store_b128(v, rs, off, 0, 16); }
DEVI __amdgpu_buffer_rsrc_t mkrsrc(const void* p, u32 bytes) {
  return __builtin_amdgcn_make_buffer_rsrc((void*)p, 0, bytes, 0x00020000);
}
#define WAITV(n) asm volatile("s_waitcnt vmcnt(" #n ")" ::: "memory")
#define WAITL(n) asm volatile("s_waitcnt lgkmcnt(" #n ")" ::: "memory")
#define BAR() __builtin_amdgcn_s_barrier()
DEVI f32x16 mfma32(u32x4 a, u32x4 b, f32x16 c) {
  return __builtin_amdgcn_mfma_f32_32x32x16_bf16(__builtin_bit_cast(bf8, a), __builtin_bit_cast(bf8, b), c, 0, 0, 0);
}
DEVI f32x4 mfma16(u32x4 a, u32x4 b, f32x4 c) {
  return __builtin_amdgcn_mfma_f32_16x16x32_bf16(__builtin_bit_cast(bf8, a), __builtin_bit_cast(bf8, b), c, 0, 0, 0);
}
DEVI float dot2(u32 a, u32 b, float c) {
  return __builtin_amdgcn_fdot2_f32_bf16(__builtin_bit_cast(bf2, a), __builtin_bit_cast(bf2, b), c, false);
}

constexpr int K1_LN = 64, K1_KG = 576, K1_PT = 288;
constexpr int K1_LDS = 38 * 1024;

struct K1Args {
  const u16 *x, *Are, *Aim, *Bre, *Bim, *Cre, *Cim, *ldt, *w1, *w3;
  char* ws;
};

__global__ __launch_bounds__(256) void k1_pre(K1Args a) {
  extern __shared__ __attribute__((aligned(16))) char smem_[];
  LDS_AS char* smem = (LDS_AS char*)smem_;
  const int tid = threadIdx.x, lane = tid & 63, wid = tid >> 6;
  const int b = blockIdx.x;
  if (b < K1_LN) {
    const int rb = (b & 7) * 8 + (b >> 3);
    const int t0 = rb * 32;
    constexpr int PITCH = 1152 + 32;
    const u32x4* src = (const u32x4*)(a.x + (size_t)t0 * D);
    u32x4 v[9];
#pragma unroll
    for (int i = 0; i < 9; i++) v[i] = src[tid + 256 * i];
#pragma unroll
    for (int i = 0; i < 9; i++) {
      int c = tid + 256 * i, r = c / 72, cc = c - r * 72;
      *(LDS_AS u32x4*)(smem + r * PITCH + cc * 16) = v[i];
    }
    if (b == 0) {
      u32x4 z = {0, 0, 0, 0};
      auto rsf = mkrsrc(a.ws + WS_FLAG, (u32)FLAG_BYTES);
#pragma unroll
      for (int i = 0; i < 8; i++) st16_wt(rsf, (tid + 256 * i) * 16, z);
    }
    __syncthreads();
    const int r = tid >> 3, sub = tid & 7;
    float xs[72];
#pragma unroll
    for (int i = 0; i < 9; i++) {
      u32x4 q = *(LDS_AS u32x4*)(smem + r * PITCH + (sub + 8 * i) * 16);
#pragma unroll
      for (int j = 0; j < 4; j++) { xs[i * 8 + 2 * j] = bflo(q[j]); xs[i * 8 + 2 * j + 1] = bfhi(q[j]); }
    }
    float s = 0.f;
#pragma unroll
    for (int i = 0; i < 72; i++) s += xs[i];
    s += __shfl_xor(s, 1); s += __shfl_xor(s, 2); s += __shfl_xor(s, 4);
    const float mean = s * (1.0f / D);
    float q2 = 0.f;
#pragma unroll
    for (int i = 0; i < 72; i++) { float dl = xs[i] - mean; q2 = fmaf(dl, dl, q2); }
    q2 += __shfl_xor(q2, 1); q2 += __shfl_xor(q2, 2); q2 += __shfl_xor(q2, 4);
    const float mb = rbf(mean);
    const float sd = rbf(sqrtf(rbf(rbf(q2 * (1.0f / D)) + 1e-5f)));
    LDS_AS float* st = (LDS_AS float*)(smem + 32 * PITCH);
    if (sub == 0) { st[2 * r] = mb; st[2 * r + 1] = sd; }
    __syncthreads();
    const int g = lane >> 4, i16 = lane & 15, q4 = i16 >> 2, p4 = i16 & 3;
    float mrow[8], srow[8];
#pragma unroll
    for (int j = 0; j < 8; j++) { mrow[j] = st[2 * (8 * g + j)]; srow[j] = st[2 * (8 * g + j) + 1]; }
    auto rsu = mkrsrc(a.ws + WS_UT, D * L * 2);
    auto rsx = mkrsrc(a.ws + WS_XT, D * L * 2);
#pragma unroll
    for (int it = 0; it < 9; it++) {
      const int cg = wid + 4 * it;
      LDS_AS char* p0 = smem + (8 * g + q4) * PITCH + cg * 32 + p4 * 8;
      u32x4 xv = tr_read2(p0, p0 + 4 * PITCH);
      u32x4 uv;
#pragma unroll
      for (int j = 0; j < 4; j++) {
        float lo = rbf(bflo(xv[j]) - mrow[2 * j]) / srow[2 * j];
        float hi = rbf(bfhi(xv[j]) - mrow[2 * j + 1]) / srow[2 * j + 1];
        uv[j] = pk2(lo, hi);
      }
      const u32 off = (u32)(cg * 16 + i16) * (L * 2) + (t0 + 8 * g) * 2;
      st16_wt(rsu, off, uv);
      st16_wt(rsx, off, xv);
    }
  } else if (b < K1_LN + K1_KG) {
    const int d = b - K1_LN;
    const int r = lane & 15, kq = lane >> 4;
    const int n0 = d * NM + 32 * wid + 8 * kq;
    const u32x4 vAre = *(const u32x4*)(a.Are + n0), vAim = *(const u32x4*)(a.Aim + n0);
    const u32x4 vBre = *(const u32x4*)(a.Bre + n0), vBim = *(const u32x4*)(a.Bim + n0);
    const u32x4 vCre = *(const u32x4*)(a.Cre + n0), vCim = *(const u32x4*)(a.Cim + n0);
    const float dt = rbf(__expf(bf2f(a.ldt[d])));
    const float p0 = (float)r;
    const float p1 = (float)(r < 8 ? 256 + 2 * r : 512 + 256 * ((r - 8) >> 2) + 4 * ((r - 8) & 3));
    const float p2 = (float)(r < 8 ? 1024 + 256 * (r >> 1) + 8 * (r & 1) : 2048);
    const float pY = (float)(16 * r);
    f32x4 acc0 = {0, 0, 0, 0}, acc1 = acc0, acc2 = acc0;
#pragma unroll
    for (int s = 0; s < 8; s++) {
      auto pick = [&](const u32x4& v) { u32 w = v[s >> 1]; return (s & 1) ? bfhi(w) : bflo(w); };
      const float ar = dt * pick(vAre) * 1.44269504088896341f;
      const float ai = dt * pick(vAim) * 0.15915494309189535f;
      const float br = dt * pick(vBre), bi = dt * pick(vBim), cr = pick(vCre), ci = pick(vCim);
      const float gr = cr * br - ci * bi, gi = cr * bi + ci * br;
      auto zpow = [&](float p, float& re, float& im) {
        float e = __builtin_amdgcn_exp2f(ar * p);
        float ph = __builtin_amdgcn_fractf(ai * p);
        re = e * __builtin_amdgcn_cosf(ph); im = e * __builtin_amdgcn_sinf(ph);
      };
      float yr, yi, x0r, x0i, x1r, x1i, x2r, x2i;
      zpow(pY, yr, yi); zpow(p0, x0r, x0i); zpow(p1, x1r, x1i); zpow(p2, x2r, x2i);
      const float Yr = gr * yr - gi * yi, nYi = -(gr * yi + gi * yr);
      acc0 = __builtin_amdgcn_mfma_f32_16x16x4f32(x0r, Yr, acc0, 0, 0, 0);
      acc1 = __builtin_amdgcn_mfma_f32_16x16x4f32(x1r, Yr, acc1, 0, 0, 0);
      acc2 = __builtin_amdgcn_mfma_f32_16x16x4f32(x2r, Yr, acc2, 0, 0, 0);
      acc0 = __builtin_amdgcn_mfma_f32_16x16x4f32(x0i, nYi, acc0, 0, 0, 0);
      acc1 = __builtin_amdgcn_mfma_f32_16x16x4f32(x1i, nYi, acc1, 0, 0, 0);
      acc2 = __builtin_amdgcn_mfma_f32_16x16x4f32(x2i, nYi, acc2, 0, 0, 0);
    }
    LDS_AS float* part = (LDS_AS float*)smem;
    *(LDS_AS f32x4*)(part + ((wid * 3 + 0) * 16 + r) * 16 + 4 * kq) = acc0;
    *(LDS_AS f32x4*)(part + ((wid * 3 + 1) * 16 + r) * 16 + 4 * kq) = acc1;
    *(LDS_AS f32x4*)(part + ((wid * 3 + 2) * 16 + r) * 16 + 4 * kq) = acc2;
    __syncthreads();
    LDS_AS float* tab = part + 4 * 768;
#pragma unroll
    for (int i = 0; i < 3; i++) {
      int e = tid + 256 * i;
      tab[e] = (part[e] + part[768 + e]) + (part[2 * 768 + e] + part[3 * 768 + e]);
    }
    __syncthreads();
    auto rsk = mkrsrc(a.ws + WS_KREV + (size_t)d * KREV_PITCH, KREV_PITCH);
    float kv[8];
#pragma unroll
    for (int j = 0; j < 8; j++) {
      const int l = 2047 - (tid * 8 + j);
      const int lr = (int)rbf((float)l);
      int tile, row, col;
      if (lr < 256) { tile = 0; row = lr & 15; col = lr >> 4; }
      else if (lr < 512) { int x = lr - 256; tile = 1; row = (x & 15) >> 1; col = x >> 4; }
      else if (lr < 1024) { int x = lr - 512; tile = 1; row = 8 + 4 * (x >> 8) + ((x & 15) >> 2); col = (x & 255) >> 4; }
      else if (lr < 2048) { int x = lr - 1024; tile = 2; row = 2 * (x >> 8) + ((x & 15) >> 3); col = (x & 255) >> 4; }
      else { tile = 2; row = 8; col = 0; }
      kv[j] = tab[(tile * 16 + col) * 16 + row];
    }
    u32x4 o = {pk2(kv[0], kv[1]), pk2(kv[2], kv[3]), pk2(kv[4], kv[5]), pk2(kv[6], kv[7])};
    st16_wt(rsk, tid * 16, o);
    if (tid < 16) { u32x4 z = {0, 0, 0, 0}; st16_wt(rsk, 4096 + tid * 16, z); }
  } else {
    const int pb = b - K1_LN - K1_KG;
    const int fq = pb >> 2, kq = pb & 3;
    u32x4 v[9];
#pragma unroll
    for (int i = 0; i < 9; i++) {
      int c = tid + 256 * i, k = c >> 4, ch = c & 15, mat = ch >> 3;
      const u16* src = (mat ? a.w3 : a.w1) + (size_t)(kq * 144 + k) * F + fq * 64 + (ch & 7) * 8;
      v[i] = __builtin_nontemporal_load((const u32x4*)src);
    }
#pragma unroll
    for (int i = 0; i < 9; i++) {
      int c = tid + 256 * i, k = c >> 4, ch = c & 15;
      *(LDS_AS u32x4*)(smem + k * 256 + (((ch >> 1) ^ (k & 3)) * 32) + (ch & 1) * 16) = v[i];
    }
    __syncthreads();
    const int P = fq * 4 + wid;
    const int hh = lane >> 5, mat = (lane >> 4) & 1, q4 = (lane & 15) >> 2, p4 = lane & 3;
    auto rsw = mkrsrc(a.ws + WS_WT + (size_t)P * 36 * 1024, 36 * 1024);
    float cs = 0.f;
#pragma unroll
    for (int kk = 0; kk < 9; kk++) {
      LDS_AS char* p0 = smem + (kk * 16 + 8 * hh + q4) * 256 + (((mat * 4 + wid) ^ q4) * 32) + p4 * 8;
      u32x4 fr = tr_read2(p0, p0 + 4 * 256);
#pragma unroll
      for (int j = 0; j < 4; j++) cs += bflo(fr[j]) + bfhi(fr[j]);
      st16_wt(rsw, (kq * 9 + kk) * 1024 + lane * 16, fr);
    }
    cs += __shfl_xor(cs, 32);
    if (lane < 32) {
      float* csp = (float*)(a.ws + WS_CSP) + ((size_t)kq * 288 + P) * 32 + lane;
      __hip_atomic_store(csp, cs, __ATOMIC_RELAXED, __HIP_MEMORY_SCOPE_AGENT);
    }
  }
}

constexpr int K2_CV = 576, K2_PT = 144, K2_CS = 8;
constexpr int CV_KBYTES = 8 * 4352;
constexpr int CV_UOFF = CV_KBYTES;
constexpr int K2_LDS = CV_KBYTES + 4608;

struct K2Args {
  const u16 *dskip, *w2;
  char* ws;
};

__global__ __launch_bounds__(256) void k2_conv(K2Args a) {
  extern __shared__ __attribute__((aligned(16))) char smem_[];
  LDS_AS char* smem = (LDS_AS char*)smem_;
  const int tid = threadIdx.x, lane = tid & 63, wid = tid >> 6;
  const int b = blockIdx.x;
  if (b < K2_CV) {
    const int d = b;
    const u32x4* kr = (const u32x4*)(a.ws + WS_KREV + (size_t)d * KREV_PITCH);
    const u32x4 k0 = kr[tid], k1 = kr[tid + 1];
    const u32x4 uv = *(const u32x4*)(a.ws + WS_UT + (size_t)d * (L * 2) + tid * 16);
    const u32x4 xv = *(const u32x4*)(a.ws + WS_XT + (size_t)d * (L * 2) + tid * 16);
    const float dsk = bf2f(a.dskip[d]);
    {
      u32 w[8] = {k0.x, k0.y, k0.z, k0.w, k1.x, k1.y, k1.z, k1.w};
#pragma unroll
      for (int e = 0; e < 8; e++) {
        u32x4 o;
#pragma unroll
        for (int j = 0; j < 4; j++)
          o[j] = (e & 1) ? __builtin_amdgcn_alignbyte(w[(e >> 1) + j + 1], w[(e >> 1) + j], 2) : w[(e >> 1) + j];
        const int c = 7 - e;
        *(LDS_AS u32x4*)(smem + c * 4352 + ((c & 1) + 4 * (c >> 1)) * 16 + tid * 16) = o;
      }
      u32x4 z = {0, 0, 0, 0};
      if (tid < 8) *(LDS_AS u32x4*)(smem + tid * 4352 + ((tid & 1) + 4 * (tid >> 1)) * 16 + 4096) = z;
      if (tid < 32) *(LDS_AS u32x4*)(smem + CV_UOFF + tid * 16) = z;
      *(LDS_AS u32x4*)(smem + CV_UOFF + 512 + tid * 16) = uv;
    }
    __syncthreads();
    const int i16 = lane & 15, g = lane >> 4, c8 = i16 & 7;
    LDS_AS char* abase = smem + c8 * 4352 + ((c8 & 1) + 4 * (c8 >> 1)) * 16 + (253 + g - (i16 >> 3)) * 16;
    LDS_AS char* bbase = smem + CV_UOFF + (2 * i16 + g + 30) * 16;
    f32x4 acc[8];
#pragma unroll
    for (int n = 0; n < 8; n++) acc[n] = (f32x4){0, 0, 0, 0};
#pragma unroll
    for (int ph = 0; ph < 2; ph++) {
      const int rho = 2 * wid + ph;
      u32x4 A[8], B[8];
#pragma unroll
      for (int j = 0; j < 8; j++) {
        A[j] = *(LDS_AS u32x4*)(abase - 64 * rho - 512 * j);
        B[j] = *(LDS_AS u32x4*)(bbase - 64 * rho + 512 * j);
      }
#pragma unroll
      for (int a1 = 0; a1 < 8; a1++)
#pragma unroll
        for (int n = a1; n < 8; n++) acc[n] = mfma16(A[a1], B[n - a1], acc[n]);
    }
    __syncthreads();
#pragma unroll
    for (int n = 0; n < 8; n++) *(LDS_AS f32x4*)(smem + ((wid * 8 + n) * 64 + lane) * 16) = acc[n];
    __syncthreads();
    {
      const int n = tid >> 5, c = (tid & 31) >> 1, hf = tid & 1;
      f32x4 y0 = {0, 0, 0, 0}, y1 = y0;
#pragma unroll
      for (int w = 0; w < 4; w++) {
        y0 += *(LDS_AS f32x4*)(smem + ((w * 8 + n) * 64 + c + 32 * hf) * 16);
        y1 += *(LDS_AS f32x4*)(smem + ((w * 8 + n) * 64 + c + 32 * hf + 16) * 16);
      }
      float y[8] = {y0[0], y0[1], y0[2], y0[3], y1[0], y1[1], y1[2], y1[3]};
      u32x4 o;
#pragma unroll
      for (int j = 0; j < 4; j++) {
        float lo = bflo(xv[j]) + rbf(rbf(y[2 * j]) + rbf(bflo(uv[j]) * dsk));
        float hi = bfhi(xv[j]) + rbf(rbf(y[2 * j + 1]) + rbf(bfhi(uv[j]) * dsk));
        o[j] = pk2(lo, hi);
      }
      auto rsh = mkrsrc(a.ws + WS_HT + (size_t)d * (L * 2), L * 2);
      st16_wt(rsh, tid * 16, o);
    }
  } else if (b < K2_CV + K2_PT) {
    const int sg = b - K2_CV;
    constexpr int PITCH = 1152 + 32;
    const u32x4* src = (const u32x4*)(a.w2 + (size_t)sg * 32 * D);
    u32x4 v[9];
#pragma unroll
    for (int i = 0; i < 9; i++) v[i] = __builtin_nontemporal_load(src + tid + 256 * i);
#pragma unroll
    for (int i = 0; i < 9; i++) {
      int c = tid + 256 * i, r = c / 72, cc = c - r * 72;
      *(LDS_AS u32x4*)(smem + r * PITCH + cc * 16) = v[i];
    }
    __syncthreads();
    const int g = lane >> 4, q4 = (lane & 15) >> 2, p4 = lane & 3;
    const int row0 = 16 * (g >> 1) + 4 * (g & 1) + q4;
    auto rsw = mkrsrc(a.ws + WS_W2T, (u32)W2T_BYTES);
#pragma unroll
    for (int nf = 0; nf < 9; nf++) {
      LDS_AS char* p0 = smem + row0 * PITCH + (wid * 9 + nf) * 32 + p4 * 8;
      u32x4 fr = tr_read2(p0, p0 + 8 * PITCH);
      st16_wt(rsw, (u32)(((wid * 144 + sg) * 9 + nf) * 1024 + lane * 16), fr);
    }
  } else {
    const int cb = b - K2_CV - K2_PT;
    const float* csp = (const float*)(a.ws + WS_CSP);
    float* cs = (float*)(a.ws + WS_CS);
    for (int e = cb * 1152 + tid; e < (cb + 1) * 1152; e += 256) {
      float s = (csp[e] + csp[9216 + e]) + (csp[2 * 9216 + e] + csp[3 * 9216 + e]);
      int ft = e / 288, o = e - ft * 288;
      __hip_atomic_store(cs + ft * 512 + o, s, __ATOMIC_RELAXED, __HIP_MEMORY_SCOPE_AGENT);
    }
  }
}

constexpr int G1_STAGE = 24576, G1_RING = 6 * G1_STAGE;
constexpr int G1_CSOFF = G1_RING;
constexpr int G1_LDS = G1_RING + 2048;

struct K3Args { char* ws; };

template <int PANEL>
DEVI void g1_epilogue(const f32x16& acc, LDS_AS char* smem, int hh, float mu, float rs, float nrl2,
                      __amdgpu_buffer_rsrc_t rsh, u32 voff, int ft) {
  LDS_AS float* cs = (LDS_AS float*)(smem + G1_CSOFF) + PANEL * 32 + 4 * hh;
  float o[8];
#pragma unroll
  for (int h2 = 0; h2 < 2; h2++) {
    const f32x4 cg = *(LDS_AS f32x4*)(cs + 8 * h2), cu = *(LDS_AS f32x4*)(cs + 16 + 8 * h2);
#pragma unroll
    for (int j = 0; j < 4; j++) {
      const float tg = fmaf(-mu, cg[j], acc[4 * h2 + j]);
      const float tu = fmaf(-mu, cu[j], acc[8 + 4 * h2 + j]);
      const float e = __builtin_amdgcn_exp2f(tg * nrl2);
      const float r = __builtin_amdgcn_rcpf(1.0f + e);
      o[4 * h2 + j] = (tg * rs) * (tu * rs) * r;
    }
  }
  u32x4 v = {pk2(o[0], o[1]), pk2(o[2], o[3]), pk2(o[4], o[5]), pk2(o[6], o[7])};
  const int P = ft * 9 + PANEL;
  st16_wt(rsh, voff + (u32)(P >> 1) * 1024 + (u32)(P & 1) * 512, v);
}

struct G1Bases { LDS_AS char* b[3]; };
template <int OFF>
DEVI u32x4 g1_ldw(const G1Bases& wb) { return *(LDS_AS u32x4*)(wb.b[OFF / 49152] + OFF % 49152); }

template <int REGION, int K>
struct G1PassStep {
  static DEVI void run(f32x16& acc, u32x4 (&w)[4], const u32x4 (&act)[36], const G1Bases& wb) {
    u32x4 cur = w[K & 3];
    acc = mfma32(cur, act[K], acc);
    OPAQUE(acc);
    if constexpr (K + 4 < 36) w[K & 3] = g1_ldw<REGION * 36864 + (K + 4) * 1024>(wb);
    PIN();
    if constexpr (K + 1 < 36) G1PassStep<REGION, K + 1>::run(acc, w, act, wb);
  }
};
template <int REGION>
DEVI f32x16 g1_pass(const u32x4 (&act)[36], const G1Bases& wb) {
  f32x16 acc = {};
  u32x4 w[4];
  w[0] = g1_ldw<REGION * 36864>(wb); w[1] = g1_ldw<REGION * 36864 + 1024>(wb);
  w[2] = g1_ldw<REGION * 36864 + 2048>(wb); w[3] = g1_ldw<REGION * 36864 + 3072>(wb);
  PIN();
  G1PassStep<REGION, 0>::run(acc, w, act, wb);
  return acc;
}

struct G1Ctx {
  const char *hT, *Wt;
  u32 aoff, loff;
  u32 lds0;
  int wid;
  G1Bases wb, ab;
};
DEVI void g1_stage_dma(const G1Ctx& c, int s) {
  const u32 dst = c.lds0 + (s % 6) * G1_STAGE + c.wid * 1024;
  dma16(c.hT + (size_t)s * 32 * (L * 2), c.aoff, dst);
  dma16(c.hT + (size_t)(s * 32 + 16) * (L * 2), c.aoff, dst + 8192);
  dma16(c.Wt + (size_t)(c.wid >> 1) * 36864 + (c.wid & 1) * 1024 + s * 2048, c.loff, dst + 16384);
}
DEVI void g1_chunk_dma(const G1Ctx& c, int ch) {
  const u32 dst = c.lds0 + ch * G1_STAGE + c.wid * 1024;
#pragma unroll
  for (int i = 0; i < 3; i++) dma16(c.Wt + 4 * 36864 + ch * G1_STAGE + c.wid * 1024 + i * 8192, c.loff, dst + i * 8192);
}
template <int J>
DEVI u32x4 g1_lda(const G1Ctx& c) {
  constexpr int OFF = ((J >> 1) % 6) * G1_STAGE + (J & 1) * 8192;
  LDS_AS char* p = c.ab.b[OFF / 49152] + OFF % 49152;
  return tr_read2(p, p + 2048);
}
template <int J, int P>
DEVI u32x4 g1_ldw1(const G1Ctx& c) { return g1_ldw<((J >> 1) % 6) * G1_STAGE + 16384 + P * 2048 + (J & 1) * 1024>(c.wb); }

template <int J>
struct G1P1Step {
  static DEVI void run(const G1Ctx& c, f32x16 (&acc)[4], u32x4 (&w)[4], u32x4 (&act)[36], float& s1, float& s2) {
    constexpr int S = J >> 1;
    if constexpr (J & 1) {
      WAITV(9);
      BAR();
      if constexpr (S + 5 < 18) g1_stage_dma(c, S + 5); else g1_chunk_dma(c, S + 5 - 18);
    }
    if constexpr (J + 1 < 36) act[J + 1] = g1_lda<J + 1>(c);
    PIN();
#define G1_ONE(P)                                              \
    { u32x4 cur = w[P];                                        \
      acc[P] = mfma32(cur, act[J], acc[P]);                    \
      if constexpr (J + 1 < 36) w[P] = g1_ldw1<J + 1, P>(c);   \
      s1 = dot2(act[J][P], 0x3f803f80u, s1); s2 = dot2(act[J][P], act[J][P], s2); \
      PIN(); }
    G1_ONE(0) G1_ONE(1) G1_ONE(2) G1_ONE(3)
#undef G1_ONE
    if constexpr (J + 1 < 36) G1P1Step<J + 1>::run(c, acc, w, act, s1, s2);
  }
};

__global__ __launch_bounds__(512, 2) void k3_gemm1(K3Args a) {
  extern __shared__ __attribute__((aligned(16))) char smem_[];
  LDS_AS char* smem = (LDS_AS char*)smem_;
  const int tid = threadIdx.x, lane = tid & 63;
  const int wid = __builtin_amdgcn_readfirstlane(tid >> 6);
  const int b = blockIdx.x, xcd = b & 7, rr = b >> 3;
  const int mt = (xcd & 1) * 4 + (rr & 3), ft = (xcd >> 1) * 8 + (rr >> 2);
  G1Ctx c;
  c.wid = wid;
  c.lds0 = (u32)(size_t)smem;
  c.hT = a.ws + WS_HT + (size_t)(2 * wid) * (L * 2) + mt * 512;
  c.Wt = a.ws + WS_WT + (size_t)ft * 9 * 36864;
  const int arow = lane >> 5, k3a = (2 * wid + arow) & 3;
  c.aoff = arow * (L * 2) + (((lane & 31) ^ (k3a << 2)) * 16);
  c.loff = lane * 16;
  dma4(a.ws + WS_CS + ft * 2048 + wid * 256, lane * 4, c.lds0 + G1_CSOFF + wid * 256);
#pragma unroll
  for (int s = 0; s < 5; s++) g1_stage_dma(c, s);

  const int hh = lane >> 5, blk = (lane >> 4) & 1, q4 = (lane & 15) >> 2, p4 = lane & 3;
#pragma unroll
  for (int i = 0; i < 3; i++) {
    c.ab.b[i] = smem + (8 * hh + q4) * 512 + ((wid ^ q4) * 64) + blk * 32 + p4 * 8 + i * 49152;
    c.wb.b[i] = smem + lane * 16 + i * 49152;
    OPAQUE(c.ab.b[i]); OPAQUE(c.wb.b[i]);
  }
  u32x4 act[36], w[4];
  f32x16 acc[4] = {};
  float s1 = 0.f, s2 = 0.f;
  WAITV(12);
  BAR();
  act[0] = g1_lda<0>(c);
  w[0] = g1_ldw1<0, 0>(c); w[1] = g1_ldw1<0, 1>(c); w[2] = g1_ldw1<0, 2>(c); w[3] = g1_ldw1<0, 3>(c);
  PIN();
  G1P1Step<0>::run(c, acc, w, act, s1, s2);
  s1 += __shfl_xor(s1, 32); s2 += __shfl_xor(s2, 32);
  const float mean = s1 * (1.0f / D);
  const float var = fmaxf(s2 * (1.0f / D) - mean * mean, 0.f);
  const float mu = rbf(mean);
  const float sd = rbf(sqrtf(rbf(rbf(var) + 1e-5f)));
  const float rs = 1.0f / sd;
  const float nrl2 = -rs * 1.44269504088896341f;
  auto rsh = mkrsrc(a.ws + WS_HFF, (u32)HFF_BYTES);
  const int m = mt * 256 + wid * 32 + (lane & 31);
  const u32 voff = (u32)(m >> 4) * (144 * 1024) + (u32)((m & 15) + 16 * hh) * 16;

  WAITV(9);
  BAR();
  g1_chunk_dma(c, 5);
  const G1Bases& wb = c.wb;
#define EPI(P, A) g1_epilogue<P>(A, smem, hh, mu, rs, nrl2, rsh, voff, ft); PIN()
  const bool early = wid < 4;
  EPI(0, acc[0]);
  if (early) { EPI(1, acc[1]); }
  acc[0] = g1_pass<0>(act, wb);
  if (!early) { EPI(1, acc[1]); }
  WAITV(11); BAR();
#pragma unroll
  for (int i = 0; i < 5; i++) {
    const int fr = (wid + 8 * i < 36) ? wid + 8 * i : wid;
    dma16(c.Wt + 8 * 36864 + fr * 1024, c.loff, c.lds0 + fr * 1024);
  }
  if (early) { EPI(2, acc[2]); EPI(3, acc[3]); }
  acc[1] = g1_pass<1>(act, wb);
  if (!early) { EPI(2, acc[2]); EPI(3, acc[3]); }
  WAITV(12); BAR();
  if (early) { EPI(4, acc[0]); EPI(5, acc[1]); }
  acc[2] = g1_pass<2>(act, wb);
  if (!early) { EPI(4, acc[0]); EPI(5, acc[1]); }
  WAITV(11); BAR();
  if (early) { EPI(6, acc[2]); }
  acc[3] = g1_pass<3>(act, wb);
  if (!early) { EPI(6, acc[2]); EPI(7, acc[3]); }
  WAITV(5); BAR();
  if (early) { EPI(7, acc[3]); }
  acc[0] = g1_pass<0>(act, wb);
  EPI(8, acc[0]);
#undef EPI
}

constexpr int G2_STAGE = 34816, G2_NST = 4, G2_LDS = G2_NST * G2_STAGE;
struct K4Args { char* ws; u16* out; };

template <int NF, int NF0>
DEVI void g2_main(LDS_AS char* smem, int lane, int mw, f32x4 (&acc)[2][5], const char* asrc, const char* wsrc, int wid) {
  const u32 lds0 = (u32)(size_t)smem, loff = lane * 16;
  const bool extra = wid < 2;
  auto stage_dma = [&](int s) {
    const u32 dst = lds0 + (s & 3) * G2_STAGE;
    dma16(asrc + s * 2048, loff, dst + wid * 2048);
    dma16(asrc + s * 2048 + 1024, loff, dst + wid * 2048 + 1024);
    dma16(wsrc + (size_t)s * 18432, loff, dst + 16384 + wid * 1024);
    dma16(wsrc + (size_t)s * 18432 + 8192, loff, dst + 16384 + 8192 + wid * 1024);
    if (extra) dma16(wsrc + (size_t)s * 18432 + 16384, loff, dst + 16384 + 16384 + wid * 1024);
  };
  stage_dma(0); stage_dma(1); stage_dma(2);
  LDS_AS char* ab = smem + mw * 4096 + lane * 16;
  LDS_AS char* wb = smem + 16384 + NF0 * 1024 + lane * 16;
  for (int s = 0; s < 18; s++) {
    if (s < 16) { if (extra) WAITV(10); else WAITV(8); }
    else if (s == 16) { WAITV(4); }
    else { WAITV(0); }
    BAR();
    if (s + 3 < 18) stage_dma(s + 3);
    const int so = (s & 3) * G2_STAGE;
#pragma unroll
    for (int kk = 0; kk < 2; kk++) {
      u32x4 a0 = *(LDS_AS u32x4*)(ab + so + kk * 1024);
      u32x4 a1 = *(LDS_AS u32x4*)(ab + so + 2048 + kk * 1024);
#pragma unroll
      for (int nf = 0; nf < NF; nf++) {
        u32x4 w = *(LDS_AS u32x4*)(wb + so + kk * 9216 + nf * 1024);
        acc[0][nf] = mfma16(w, a0, acc[0][nf]);
        acc[1][nf] = mfma16(w, a1, acc[1][nf]);
      }
    }
    WAITL(0);
  }
}

__global__ __launch_bounds__(512, 2) void k4_gemm2(K4Args a) {
  extern __shared__ __attribute__((aligned(16))) char smem_[];
  LDS_AS char* smem = (LDS_AS char*)smem_;
  const int tid = threadIdx.x, lane = tid & 63;
  const int wid = __builtin_amdgcn_readfirstlane(tid >> 6);
  const int b = blockIdx.x, xcd = b & 7, rr = b >> 3;
  const int ks = xcd >> 1, mt = (xcd & 1) * 8 + (rr & 7), nt = rr >> 3;
  const int tile = mt * 4 + nt;
  const int mw = wid & 3, nh = wid >> 2;
  const char* asrc = a.ws + WS_HFF + ((size_t)(mt * 8 + wid) * 144 + ks * 36) * 1024;
  const char* wsrc = a.ws + WS_W2T + ((size_t)nt * 144 + ks * 36) * 9216 + wid * 1024;
  const auto rs_out = mkrsrc(a.ws + WS_PART + ((size_t)(tile * 4 + mw) * 4 + ks) * 18432, 18432);
  const auto rs_in = mkrsrc(a.ws + WS_PART + (size_t)(tile * 4 + ks) * 4 * 18432, 4 * 18432);
  u32* cnt_out = (u32*)(a.ws + WS_FLAG + (size_t)(tile * 4 + mw) * 128);
  u32* cnt_in = (u32*)(a.ws + WS_FLAG + (size_t)(tile * 4 + ks) * 128);
  f32x4 acc[2][5];
#pragma unroll
  for (int i = 0; i < 2; i++)
#pragma unroll
    for (int j = 0; j < 5; j++) acc[i][j] = (f32x4){0, 0, 0, 0};
  if (nh == 0) g2_main<5, 0>(smem, lane, mw, acc, asrc, wsrc, wid);
  else g2_main<4, 5>(smem, lane, mw, acc, asrc, wsrc, wid);

  const int nfn = nh ? 4 : 5, nf0 = nh ? 5 : 0;
  BAR();
  if (mw == ks) {
#pragma unroll
    for (int i = 0; i < 2; i++)
#pragma unroll
      for (int j = 0; j < 5; j++)
        if (j < nfn) *(LDS_AS f32x4*)(smem + ((i * 9 + nf0 + j) * 64 + lane) * 16) = acc[i][j];
  } else {
#pragma unroll
    for (int i = 0; i < 2; i++)
#pragma unroll
      for (int j = 0; j < 5; j++)
        if (j < nfn) st16_wt(rs_out, ((i * 9 + nf0 + j) * 64 + lane) * 16, __builtin_bit_cast(u32x4, acc[i][j]));
    WAITV(0);
    if (lane == 0) __hip_atomic_fetch_add(cnt_out, 1u, __ATOMIC_RELAXED, __HIP_MEMORY_SCOPE_AGENT);
  }
  const u16* hT = (const u16*)(a.ws + WS_HT);
  float res[3][4];
  const int ntl = wid < 2 ? 3 : 2;
#pragma unroll
  for (int i = 0; i < 3; i++) {
    if (i < ntl) {
      const int t = wid + 8 * i, mf2 = t / 9, nf = t - mf2 * 9;
      const int m = mt * 128 + ks * 32 + mf2 * 16 + (lane & 15), n = nt * 144 + nf * 16 + 4 * (lane >> 4);
#pragma unroll
      for (int j = 0; j < 4; j++) res[i][j] = bf2f(hT[(size_t)(n + j) * L + m]);
    }
  }
  if (wid == 0) {
    u32 spins = 0;
    while (__builtin_amdgcn_readfirstlane(__hip_atomic_load(cnt_in, __ATOMIC_RELAXED, __HIP_MEMORY_SCOPE_AGENT)) < 6u) {
      __builtin_amdgcn_s_sleep(1);
      if (++spins > (1u << 26)) break;
    }
  }
  __syncthreads();
#pragma unroll
  for (int i = 0; i < 3; i++) {
    if (i < ntl) {
      const int t = wid + 8 * i, mf2 = t / 9, nf = t - mf2 * 9;
      const u32 o16 = (t * 64 + lane) * 16;
      f32x4 v = *(LDS_AS f32x4*)(smem + o16);
      f32x4 p1 = __builtin_bit_cast(f32x4, __builtin_amdgcn_raw_buffer_load_b128(rs_in, ((ks + 1) & 3) * 18432 + o16, 0, 16));
      f32x4 p2 = __builtin_bit_cast(f32x4, __builtin_amdgcn_raw_buffer_load_b128(rs_in, ((ks + 2) & 3) * 18432 + o16, 0, 16));
      f32x4 p3 = __builtin_bit_cast(f32x4, __builtin_amdgcn_raw_buffer_load_b128(rs_in, ((ks + 3) & 3) * 18432 + o16, 0, 16));
      v = (v + p1) + (p2 + p3);
      const int m = mt * 128 + ks * 32 + mf2 * 16 + (lane & 15), n = nt * 144 + nf * 16 + 4 * (lane >> 4);
      u32x2 o = {pk2(res[i][0] + rbf(v[0]), res[i][1] + rbf(v[1])), pk2(res[i][2] + rbf(v[2]), res[i][3] + rbf(v[3]))};
      *(u32x2*)(a.out + (size_t)m * D + n) = o;
    }
  }
}

extern "C" void kernel_launch(void* const* d_in, const int* in_sizes, int n_in, void* d_out, int out_size, void* d_ws,
                              size_t ws_size, hipStream_t stream) {
  (void)in_sizes; (void)n_in; (void)out_size; (void)ws_size;
  (void)hipFuncSetAttribute((const void*)k3_gemm1, hipFuncAttributeMaxDynamicSharedMemorySize, G1_LDS);
  (void)hipFuncSetAttribute((const void*)k4_gemm2, hipFuncAttributeMaxDynamicSharedMemorySize, G2_LDS);
  K1Args a1;
  a1.x = (const u16*)d_in[0]; a1.Are = (const u16*)d_in[1]; a1.Aim = (const u16*)d_in[2];
  a1.Bre = (const u16*)d_in[3]; a1.Bim = (const u16*)d_in[4]; a1.Cre = (const u16*)d_in[5];
  a1.Cim = (const u16*)d_in[6]; a1.ldt = (const u16*)d_in[7]; a1.w1 = (const u16*)d_in[10];
  a1.w3 = (const u16*)d_in[11]; a1.ws = (char*)d_ws;
  k1_pre<<<K1_LN + K1_KG + K1_PT, 256, K1_LDS, stream>>>(a1);
  K2Args a2; a2.dskip = (const u16*)d_in[8]; a2.w2 = (const u16*)d_in[12]; a2.ws = (char*)d_ws;
  k2_conv<<<K2_CV + K2_PT + K2_CS, 256, K2_LDS, stream>>>(a2);
  K3Args a3; a3.ws = (char*)d_ws;
  k3_gemm1<<<256, 512, G1_LDS, stream>>>(a3);
  K4Args a4; a4.ws = (char*)d_ws; a4.out = (u16*)d_out;
  k4_gemm2<<<256, 512, G2_LDS, stream>>>(a4);
}
```

```cpp
#include <hip/hip_runtime.h>
#include <stdint.h>

typedef unsigned short u16;
typedef unsigned int u32;
typedef unsigned long long u64;
typedef __bf16 bf2 __attribute__((ext_vector_type(2)));
typedef __bf16 bf8 __attribute__((ext_vector_type(8)));
typedef short s4 __attribute__((ext_vector_type(4)));
typedef u32 u32x2 __attribute__((ext_vector_type(2)));
typedef u32 u32x4 __attribute__((ext_vector_type(4)));
typedef float f32x2 __attribute__((ext_vector_type(2)));
typedef float f32x4 __attribute__((ext_vector_type(4)));
typedef float f32x16 __attribute__((ext_vector_type(16)));
#define LDS_AS __attribute__((address_space(3)))
#define DEVI __device__ __forceinline__

#define NT_X
#define NT_P
constexpr int L = 2048, D = 576, NM = 128, F = 4608;

constexpr size_t WS_UX   = 0;
constexpr size_t WS_HT   = WS_UX + (size_t)D * L * 4;
constexpr int    KREV_PITCH = 4352;
constexpr size_t WS_KREV = WS_HT + (size_t)D * L * 2;
constexpr size_t WS_WT   = WS_KREV + (size_t)D * KREV_PITCH;
constexpr size_t WT_BYTES = (size_t)288 * 36 * 1024;
constexpr size_t WS_W2T  = WS_WT + WT_BYTES + 65536;
constexpr size_t W2T_BYTES = (size_t)4 * 144 * 9 * 1024;
constexpr size_t WS_CSP  = WS_W2T + W2T_BYTES + 65536;
constexpr size_t WS_HFF  = WS_CSP + (size_t)32 * 4 * 512 * 4;
constexpr size_t HFF_BYTES = (size_t)128 * 144 * 1024;
constexpr size_t WS_PART = WS_HFF + HFF_BYTES;
constexpr size_t PART_BYTES = (size_t)64 * 4 * 4 * 9216;
constexpr size_t WS_FLAG = WS_PART + PART_BYTES;
constexpr size_t FLAG_BYTES = (size_t)512 * 128;
constexpr size_t WS_STAT = WS_FLAG + FLAG_BYTES;
constexpr size_t STAT_BYTES = (size_t)8 * 18 * 256 * 8;
constexpr size_t WS_SCR = WS_STAT + STAT_BYTES;

DEVI float bf2f(u32 v) { return __uint_as_float(v << 16); }
DEVI float bflo(u32 v) { return __uint_as_float(v << 16); }
DEVI float bfhi(u32 v) { return __uint_as_float(v & 0xffff0000u); }
DEVI u32 pk2(float lo, float hi) {
  bf2 r = {(__bf16)lo, (__bf16)hi};
  return __builtin_bit_cast(u32, r);
}
DEVI float rbf(float x) { return __uint_as_float(((u32)__builtin_bit_cast(u16, (__bf16)x)) << 16); }
DEVI void dma16(const char* sbase, u32 voff, u32 lds) {
  asm volatile("s_mov_b32 m0, %2\n\ts_nop 0\n\tglobal_load_lds_dwordx4 %0, %1 sc1" ::"v"(voff), "s"(sbase), "s"(lds) : "memory");
}
#define OPAQUE(x) asm volatile("" : "+v"(x))
#define PIN() __builtin_amdgcn_sched_barrier(0)
DEVI s4 tr_read(LDS_AS void* p) { return __builtin_amdgcn_ds_read_tr16_b64_v4i16((LDS_AS s4*)p); }
DEVI u32x4 tr_read2(LDS_AS char* p0, LDS_AS char* p1) {
  u32x2 a = __builtin_bit_cast(u32x2, tr_read(p0)), b = __builtin_bit_cast(u32x2, tr_read(p1));
  u32x4 r = {a.x, a.y, b.x, b.y};
  return r;
}
DEVI void st16_wt(__amdgpu_buffer_rsrc_t rs, u32 off, u32x4 v) { __builtin_amdgcn_raw_buffer_store_b128(v, rs, off, 0, 16); }
DEVI __amdgpu_buffer_rsrc_t mkrsrc(const void* p, u32 bytes) {
  return __builtin_amdgcn_make_buffer_rsrc((void*)p, 0, bytes, 0x00020000);
}
#define WAITV(n) asm volatile("s_waitcnt vmcnt(" #n ")" ::: "memory")
#define WAITL(n) asm volatile("s_waitcnt lgkmcnt(" #n ")" ::: "memory")
#define BAR() __builtin_amdgcn_s_barrier()
DEVI f32x16 mfma32(u32x4 a, u32x4 b, f32x16 c) {
  return __builtin_amdgcn_mfma_f32_32x32x16_bf16(__builtin_bit_cast(bf8, a), __builtin_bit_cast(bf8, b), c, 0, 0, 0);
}
DEVI f32x4 mfma16(u32x4 a, u32x4 b, f32x4 c) {
  return __builtin_amdgcn_mfma_f32_16x16x32_bf16(__builtin_bit_cast(bf8, a), __builtin_bit_cast(bf8, b), c, 0, 0, 0);
}
typedef _Float16 h2v __attribute__((ext_vector_type(2)));
typedef _Float16 h8v __attribute__((ext_vector_type(8)));
DEVI u32 pkh2(float lo, float hi) { h2v r = {(_Float16)lo, (_Float16)hi}; return __builtin_bit_cast(u32, r); }
DEVI f32x4 mfma16h(u32x4 a, u32x4 b, f32x4 c) {
  return __builtin_amdgcn_mfma_f32_16x16x32_f16(__builtin_bit_cast(h8v, a), __builtin_bit_cast(h8v, b), c, 0, 0, 0);
}
template <int CTRL> DEVI float dpp(float v) {
  return __builtin_bit_cast(float, __builtin_amdgcn_update_dpp(0, __builtin_bit_cast(int, v), CTRL, 0xf, 0xf, true));
}
DEVI float sum8(float v) {
  v += dpp<0xB1>(v); v += dpp<0x4E>(v); v += dpp<0x141>(v);
  return v;
}
DEVI float dot2(u32 a, u32 b, float c) {
  return __builtin_amdgcn_fdot2_f32_bf16(__builtin_bit_cast(bf2, a), __builtin_bit_cast(bf2, b), c, false);
}

#define TS_DECL
#define TS(idx)
#define TS_OUT(kern)

#define K1ORD 1
constexpr int K1_KG = 576, K1_LN = 128, K1_PT = 176;
constexpr int K1_LDS = 38 * 1024;

struct K1Args {
  const u16 *x, *Are, *Aim, *Bre, *Bim, *Cre, *Cim, *ldt, *w1, *w3, *w2;
  char* ws;
};

constexpr int lag_index(int l) {
  if (l < 256) return l;
  int e = 0;
  while ((l >> e) >= 256) e++;
  int q = l >> e;
  const int rem = l & ((1 << e) - 1), half = 1 << (e - 1);
  if (rem > half || (rem == half && (q & 1))) q++;
  return 128 * e + q;
}
constexpr int kg_p1(int r) { return r < 8 ? 256 + 2 * r : 512 + 256 * ((r - 8) >> 2) + 4 * ((r - 8) & 3); }
constexpr int kg_p2(int r) { return r < 8 ? 1024 + 256 * (r >> 1) + 8 * (r & 1) : 2048; }
struct KgTab {
  u16 g[256][8]; u16 t[256][2];
  constexpr KgTab() : g(), t() {
    for (int i = 0; i < 256; i++) {
      for (int j = 0; j < 8; j++) g[i][j] = (u16)(4 * lag_index(2047 - (i * 8 + j)));
      const int r = i & 15, c16 = (i >> 4) * 16;
      t[i][0] = (u16)(4 * lag_index(kg_p1(r) + c16));
      t[i][1] = (u16)(4 * ((r < 8 || i == 8) ? lag_index(kg_p2(r) + c16) : 700));
    }
  }
};
__constant__ __attribute__((aligned(16))) const KgTab kg_tab{};

DEVI int panel_log(int ft, int p) { const int ls = 4 - (ft & 3); return p < ls ? p : (p < ls + 4 ? p - ls + 5 : p - 4); }
DEVI int panel_phys(int ft, int lp) { const int ls = 4 - (ft & 3); return lp < ls ? lp : (lp < 5 ? lp + 4 : lp - 5 + ls); }

template <int NT, int NV>
DEVI void retile_addr(const u16* w1, const u16* w3, const u16* w2, int unit, int tid, const u32x4* (&p)[NV]) {
  if (unit < 288) {
    const int fq = unit >> 2, kq = unit & 3, ch = tid & 15;
    const u16* src = ((ch >> 3) ? w3 : w1) + (size_t)(kq * 144) * F + fq * 64 + (ch & 7) * 8;
#pragma unroll
    for (int i = 0; i < NV; i++) {
      int k = (tid + NT * i) >> 4; k = k > 143 ? 143 : k;
      p[i] = (const u32x4*)(src + (size_t)k * F);
    }
  } else {
    const int sg = unit - 288, ks = sg / 36, q = sg - ks * 36;
    const int fa = ks * 8 + (q < 32 ? (q & 7) : 2 * (q - 32)), fb = q < 32 ? fa : fa + 1;
    const int Pa = fa * 9 + panel_phys(fa, q < 32 ? 2 * (q >> 3) : 8), Pb = fb * 9 + panel_phys(fb, q < 32 ? 2 * (q >> 3) + 1 : 8);
    const u32x4* srcA = (const u32x4*)(w2 + (size_t)Pa * 16 * D);
    const u32x4* srcB = (const u32x4*)(w2 + (size_t)Pb * 16 * D) - 1152;
#pragma unroll
    for (int i = 0; i < NV; i++) {
      int c = tid + NT * i; c = c > 2303 ? 2303 : c;
      p[i] = (c < 1152 ? srcA : srcB) + c;
    }
  }
}
template <int NT, int NV>
DEVI void retile_load(const u16* w1, const u16* w3, const u16* w2, int unit, int tid, u32x4 (&v)[NV]) {
  const u32x4* p[NV];
  retile_addr<NT, NV>(w1, w3, w2, unit, tid, p);
#pragma unroll
  for (int i = 0; i < NV; i++) v[i] = __builtin_nontemporal_load(p[i]);
}
template <int NT, int NV>
DEVI void retile_stage(LDS_AS char* smem, int unit, int tid, const u32x4 (&v)[NV]) {
  if (unit < 288) {
    const int ch = tid & 15;
#pragma unroll
    for (int i = 0; i < NV; i++) {
      int k = (tid + NT * i) >> 4; k = k > 143 ? 143 : k;
      *(LDS_AS u32x4*)(smem + k * 256 + (((ch >> 1) ^ (k & 3)) * 32) + (ch & 1) * 16) = v[i];
    }
  } else {
#pragma unroll
    for (int i = 0; i < NV; i++) {
      int c = tid + NT * i; c = c > 2303 ? 2303 : c;
      const int r = c / 72, cc = c - r * 72;
      *(LDS_AS u32x4*)(smem + r * 1152 + ((cc ^ (r & 6)) * 16)) = v[i];
    }
  }
}
struct RtDst { const char* base; u32 voff; int k0; };
DEVI u32 rt_off(const RtDst& d, int i) { return d.k0 < 0 ? i * 1024 : ((d.k0 + i) >> 1) * 2048 + ((d.k0 + i) & 1) * 512; }
DEVI RtDst retile_read(char* ws, LDS_AS char* smem, int unit, int lane, int wid, bool pub, u32x4 (&fr)[9]) {
  const int q4 = (lane & 15) >> 2, p4 = lane & 3;
  RtDst d;
  if (unit < 288) {
    const int fq = unit >> 2, kq = unit & 3;
    const int P = fq * 4 + wid, ft = P / 9, lp = panel_log(ft, P - ft * 9);
    const int mat = lane >> 5, g2 = (lane >> 4) & 1;
    float cs = 0.f;
#pragma unroll
    for (int kk = 0; kk < 9; kk++) {
      LDS_AS char* p0 = smem + (kk * 16 + 8 * g2 + q4) * 256 + (((mat * 4 + wid) ^ q4) * 32) + p4 * 8;
      fr[kk] = tr_read2(p0, p0 + 4 * 256);
#pragma unroll
      for (int j = 0; j < 4; j++) cs = dot2(fr[kk][j], 0x3f803f80u, cs);
    }
    cs += __shfl_xor(cs, 16);
    if (pub && g2 == 0) {
      float* csp = (float*)(ws + WS_CSP) + ((size_t)ft * 512 + lp * 32 + mat * 16 + (lane & 15)) * 4 + kq;
      __hip_atomic_store(csp, cs, __ATOMIC_RELAXED, __HIP_MEMORY_SCOPE_AGENT);
    }
    d.base = ws + WS_WT + (size_t)(ft * 9 + lp) * 36864; d.voff = mat * 1024 + (lane & 31) * 16; d.k0 = kq * 9;
  } else {
    const int sg = unit - 288;
    const int row0 = 8 * (lane >> 4) + q4;
#pragma unroll
    for (int nf = 0; nf < 9; nf++) {
      LDS_AS char* p0 = smem + row0 * 1152 + (((wid * 9 + nf) ^ ((row0 >> 1) & 3)) * 32) + p4 * 8;
      LDS_AS char* p1 = smem + (row0 + 4) * 1152 + (((wid * 9 + nf) ^ (((row0 + 4) >> 1) & 3)) * 32) + p4 * 8;
      fr[nf] = tr_read2(p0, p1);
    }
    d.base = ws + WS_W2T + (size_t)(wid * 144 + sg) * 9216; d.voff = lane * 16; d.k0 = -1;
  }
  return d;
}

__global__ __launch_bounds__(256, 4) void k1_pre(K1Args a) {
  extern __shared__ __attribute__((aligned(16))) char smem_[];
  LDS_AS char* smem = (LDS_AS char*)smem_;
  const int tid = threadIdx.x, lane = tid & 63;
  const int wid = __builtin_amdgcn_readfirstlane(tid >> 6);
  int b = blockIdx.x;
  TS_DECL; TS(0);
  if (b < K1_KG) {
    const int d = b;
    LDS_AS f32x4* prm = (LDS_AS f32x4*)(smem + 16384);
    if (tid < NM) {
      const int n = d * NM + tid;
#define LDP(p) bf2f(__builtin_nontemporal_load(p + n))
      const float Are = LDP(a.Are), Aim = LDP(a.Aim), Bre = LDP(a.Bre), Bim = LDP(a.Bim);
      const float cr = LDP(a.Cre), ci = LDP(a.Cim);
      const float dt = rbf(__expf(bf2f(a.ldt[d])));
      const float br = dt * Bre, bi = dt * Bim;
      f32x4 v = {dt * Are * 1.44269504088896341f,
                 dt * Aim * 0.15915494309189535f,
                 cr * br - ci * bi, cr * bi + ci * br};
      prm[tid] = v;
    }
    const int r = lane & 15, kq = lane >> 4;
    const float p0 = (float)r, p1 = (float)kg_p1(r), p2 = (float)kg_p2(r), pY = (float)(16 * r);
    const u32x4 gi = *(const u32x4*)kg_tab.g[tid];
    const u32 ti = *(const u32*)kg_tab.t[tid];
    WAITL(0); BAR(); TS(3);
    f32x4 acc0 = {0, 0, 0, 0}, acc1 = acc0, acc2 = acc0;
    const f32x4 qm = prm[32 * wid + (lane & 31)];
    if (__all(qm[0] <= 0.f && fabsf(qm[1]) <= 0.125f)) {
      float cm = fabsf(qm[2]) + fabsf(qm[3]);
      cm = fmaxf(cm, dpp<0xB1>(cm)); cm = fmaxf(cm, dpp<0x4E>(cm)); cm = fmaxf(cm, dpp<0x141>(cm)); cm = fmaxf(cm, dpp<0x140>(cm));
      const float cmax = fmaxf(__builtin_bit_cast(float, __builtin_amdgcn_readlane(__builtin_bit_cast(int, cm), 0)),
                               __builtin_bit_cast(float, __builtin_amdgcn_readlane(__builtin_bit_cast(int, cm), 16)));
      const int E = __builtin_amdgcn_frexp_expf(cmax);
      const float ey = (float)(14 - E);
      const float oscale = __builtin_amdgcn_ldexpf(1.0f, E - 28);
#pragma unroll
      for (int s = 0; s < 2; s++) {
        u32x4 x0, x1, x2, y;
#pragma unroll
        for (int m4 = 0; m4 < 4; m4++) {
          const f32x4 q = prm[32 * wid + 16 * s + 4 * kq + m4];
          const f32x2 q01 = {q[0], q[1]};
          auto zpow = [&](float p, float sc) {
            const f32x2 pp = {p, p}, ss = {sc, 0.f};
            const f32x2 ar = __builtin_elementwise_fma(q01, pp, ss);
            const float e = __builtin_amdgcn_exp2f(ar[0]);
            const f32x2 cs = {__builtin_amdgcn_cosf(ar[1]), __builtin_amdgcn_sinf(ar[1])}, ee = {e, e};
            return cs * ee;
          };
          const f32x2 yv = zpow(pY, ey);
          y[m4] = pkh2(q[2] * yv[0] - q[3] * yv[1], -(q[2] * yv[1] + q[3] * yv[0]));
          f32x2 v;
          v = zpow(p0, 14.f); x0[m4] = pkh2(v[0], v[1]);
          v = zpow(p1, 14.f); x1[m4] = pkh2(v[0], v[1]);
          v = zpow(p2, 14.f); x2[m4] = pkh2(v[0], v[1]);
        }
        acc0 = mfma16h(x0, y, acc0); acc1 = mfma16h(x1, y, acc1); acc2 = mfma16h(x2, y, acc2);
      }
      acc0 *= oscale; acc1 *= oscale; acc2 *= oscale;
    } else {
#pragma unroll 1
      for (int s = 0; s < 2; s++) {
        u32x4 xh0, xl0, xh1, xl1, xh2, xl2, yh, yl;
#pragma unroll
        for (int m4 = 0; m4 < 4; m4++) {
          const f32x4 q = prm[32 * wid + 16 * s + 4 * kq + m4];
          auto zpow = [&](float p, float& re, float& im) {
            float e = __builtin_amdgcn_exp2f(q[0] * p);
            float ph = __builtin_amdgcn_fractf(q[1] * p);
            re = e * __builtin_amdgcn_cosf(ph); im = e * __builtin_amdgcn_sinf(ph);
          };
          auto split = [&](float re, float im, u32& hi, u32& lo) {
            hi = pk2(re, im);
            lo = pk2(re - bflo(hi), im - bfhi(hi));
          };
          float yr, yi, re, im; u32 h, l;
          zpow(pY, yr, yi);
          split(q[2] * yr - q[3] * yi, -(q[2] * yi + q[3] * yr), h, l); yh[m4] = h; yl[m4] = l;
          zpow(p0, re, im); split(re, im, h, l); xh0[m4] = h; xl0[m4] = l;
          zpow(p1, re, im); split(re, im, h, l); xh1[m4] = h; xl1[m4] = l;
          zpow(p2, re, im); split(re, im, h, l); xh2[m4] = h; xl2[m4] = l;
        }
        acc0 = mfma16(xl0, yh, acc0); acc1 = mfma16(xl1, yh, acc1); acc2 = mfma16(xl2, yh, acc2);
        acc0 = mfma16(xh0, yl, acc0); acc1 = mfma16(xh1, yl, acc1); acc2 = mfma16(xh2, yl, acc2);
        acc0 = mfma16(xh0, yh, acc0); acc1 = mfma16(xh1, yh, acc1); acc2 = mfma16(xh2, yh, acc2);
      }
    }
    LDS_AS float* part = (LDS_AS float*)smem;
    *(LDS_AS f32x4*)(part + ((wid * 3 + 0) * 16 + r) * 16 + 4 * kq) = acc0;
    *(LDS_AS f32x4*)(part + ((wid * 3 + 1) * 16 + r) * 16 + 4 * kq) = acc1;
    *(LDS_AS f32x4*)(part + ((wid * 3 + 2) * 16 + r) * 16 + 4 * kq) = acc2;
    __syncthreads(); TS(4);
    LDS_AS float* tab = part + 4 * 768;
    LDS_AS char* tb = (LDS_AS char*)tab;
    tab[tid] = (part[tid] + part[768 + tid]) + (part[2 * 768 + tid] + part[3 * 768 + tid]);
    *(LDS_AS float*)(tb + (ti & 0xffffu)) = (part[256 + tid] + part[768 + 256 + tid]) + (part[2 * 768 + 256 + tid] + part[3 * 768 + 256 + tid]);
    *(LDS_AS float*)(tb + (ti >> 16)) = (part[512 + tid] + part[768 + 512 + tid]) + (part[2 * 768 + 512 + tid] + part[3 * 768 + 512 + tid]);
    __syncthreads(); TS(5);
    auto rsk = mkrsrc(a.ws + WS_KREV + (size_t)d * KREV_PITCH, KREV_PITCH);
    u32x4 o;
#pragma unroll
    for (int j = 0; j < 4; j++) o[j] = pk2(*(LDS_AS float*)(tb + (gi[j] & 0xffffu)), *(LDS_AS float*)(tb + (gi[j] >> 16)));
    st16_wt(rsk, tid * 16, o);
    if (tid < 16) { u32x4 z = {0, 0, 0, 0}; st16_wt(rsk, 4096 + tid * 16, z); }
    else if (tid < 48) { u32x4 z = {~0u, ~0u, ~0u, ~0u}; st16_wt(mkrsrc(a.ws + WS_STAT, (u32)STAT_BYTES), (d * 32 + tid - 16) * 16, z); }
    else if (tid < 56 && d < 512) { u32x4 z = {0, 0, 0, 0}; st16_wt(mkrsrc(a.ws + WS_FLAG, (u32)FLAG_BYTES), (d * 8 + tid - 48) * 16, z); }
  } else if (b < K1_KG + K1_LN) {
    __builtin_amdgcn_s_setprio(3);
    const int lb = b - K1_KG;
    const int rb = (lb >> 4) * 8 + (lb & 7), chalf = (lb >> 3) & 1;
    constexpr int PITCH = 1152 + 32;
    const u32x4* src = (const u32x4*)(a.x + (size_t)rb * 32 * D);
    u32x4 v[9];
#pragma unroll
    for (int i = 0; i < 9; i++) v[i] = __builtin_nontemporal_load(src + tid + 256 * i);
#pragma unroll
    for (int i = 0; i < 9; i++) {
      int c = tid + 256 * i, r = c / 72, cc = c - r * 72;
      *(LDS_AS u32x4*)(smem + r * PITCH + cc * 16) = v[i];
    }
    __syncthreads(); TS(3);
    const int r = tid >> 3, sub = tid & 7;
    u32x4 q[9];
    f32x4 s4v = {0, 0, 0, 0};
#pragma unroll
    for (int i = 0; i < 9; i++) {
      q[i] = *(LDS_AS u32x4*)(smem + r * PITCH + (sub + 8 * i) * 16);
#pragma unroll
      for (int j = 0; j < 4; j++) s4v[j] = dot2(q[i][j], 0x3f803f80u, s4v[j]);
    }
    const float mean = sum8((s4v[0] + s4v[1]) + (s4v[2] + s4v[3])) * (1.0f / D);
    f32x4 qa = {0, 0, 0, 0}, qb = qa;
#pragma unroll
    for (int i = 0; i < 9; i++)
#pragma unroll
      for (int j = 0; j < 4; j++) {
        const float d0 = bflo(q[i][j]) - mean, d1 = bfhi(q[i][j]) - mean;
        qa[j] = fmaf(d0, d0, qa[j]); qb[j] = fmaf(d1, d1, qb[j]);
      }
    qa += qb;
    const float q2 = sum8((qa[0] + qa[1]) + (qa[2] + qa[3]));
    LDS_AS f32x2* st = (LDS_AS f32x2*)(smem + 32 * PITCH);
    if (sub == 0) {
      const float sd = rbf(sqrtf(rbf(rbf(q2 * (1.0f / D)) + 1e-5f)));
      f32x2 o = {rbf(mean), 1.0f / sd};
      st[r] = o;
    }
    __syncthreads(); TS(4);
    const int g = lane >> 4, i16 = lane & 15, q4 = i16 >> 2, p4 = i16 & 3;
    f32x2 rst[8];
#pragma unroll
    for (int j = 0; j < 8; j++) rst[j] = st[8 * g + j];
    auto rsu = mkrsrc(a.ws + WS_UX + (size_t)rb * D * 128, D * 128);
#pragma unroll
    for (int it = 0; it < 5; it++) {
      if (wid + 4 * it >= 18) break;
      const int cg = chalf * 18 + wid + 4 * it;
      LDS_AS char* p0 = smem + (8 * g + q4) * PITCH + cg * 32 + p4 * 8;
      u32x4 xv = tr_read2(p0, p0 + 4 * PITCH);
      u32x4 uv;
#pragma unroll
      for (int j = 0; j < 4; j++) {
        const u32 dpk = pk2(bflo(xv[j]) - rst[2 * j][0], bfhi(xv[j]) - rst[2 * j + 1][0]);
        uv[j] = pk2(bflo(dpk) * rst[2 * j][1], bfhi(dpk) * rst[2 * j + 1][1]);
      }
      const u32 off = (u32)(cg * 16 + i16) * 128 + g * 16;
      st16_wt(rsu, off, uv);
      st16_wt(rsu, off + 64, xv);
    }
  } else {
    __builtin_amdgcn_s_setprio(2);
#define PT_DELAY 3
    const int pb = b - K1_LN - K1_KG;
    u32 cpf = 0;
    if (pb < 40 && wid == 0) {
      u64 pc;
      asm volatile("s_getpc_b64 s[40:41]\n\ts_add_u32 s40, s40, _Z7k2_main6K2Args@rel32@lo+4\n\ts_addc_u32 s41, s41, _Z7k2_main6K2Args@rel32@hi+12\n\ts_mov_b64 %0, s[40:41]"
                   : "=s"(pc) :: "s40", "s41", "scc");
      cpf = *(const volatile u32*)((const char*)pc + (pb >> 3) * 8192 + lane * 128);
    }
    for (int i = 0; i < PT_DELAY; i++) __builtin_amdgcn_s_sleep(8);
    asm volatile("" ::"v"(cpf));
    const int qi = pb >> 2;
    const int unit = pb < 160 ? (((qi / 5) * 9 + 2 * (qi % 5)) << 2) + (pb & 3) : 288 + 128 + (pb - 160);
    u32x4 v[9];
    retile_load<256, 9>(a.w1, a.w3, a.w2, unit, tid, v);
    retile_stage<256, 9>(smem, unit, tid, v);
    __syncthreads();
    const RtDst rd = retile_read(a.ws, smem, unit, lane, wid, true, v);
    const auto rsw = mkrsrc(rd.base, 36864);
#pragma unroll
    for (int i = 0; i < 9; i++) st16_wt(rsw, rt_off(rd, i) + rd.voff, v[i]);
  }
  TS(1);
  TS(2); TS_OUT(0);
}

constexpr int CV_CP = 4160;
constexpr int CV_UOFF = 4 * CV_CP;
constexpr int CV_BYTES = CV_UOFF + 4608;
constexpr int CV_K0 = 36864;
constexpr int RA = 0, RB = 36864, RC = 73728, RD = 110592;
constexpr int G1_RING = 36864, G1_ACT = 16384 + 128, G1_STAGE = G1_ACT + 4096;
constexpr int G1_RT = G1_RING + 4 * G1_STAGE;
constexpr int G1_CSOFF = G1_RING + 6 * G1_STAGE;
constexpr int G2_RES = 147456;
constexpr int K2_LDS = G1_CSOFF + 2048;
constexpr int g2_slot(int s) { return (s & 3) == 0 ? RD : ((s & 3) == 1 ? RA : ((s & 3) == 2 ? RB : RC)); }
constexpr int g1_region(int p) { return p == 0 || p == 6 ? RA : (p == 3 || p == 7 ? RB : (p == 4 || p == 8 ? RC : RD)); }
constexpr int FL_HFF = 32768, FL_A = 57344, FL_B = 59392, FL_RT = 61440, FL_IN = 63488;

struct K2Args { const u16 *dskip, *w1, *w3, *w2; char* ws; u16* out; };

struct G1Acc { f32x4 v[2][2]; };
struct G1Row { float mu[2], sd2[2], nrl2[2]; };
template <int PANEL>
DEVI void g1_epilogue(const G1Acc& acc, LDS_AS char* smem, int g, const G1Row& r, __amdgpu_buffer_rsrc_t rsh, u32 voff, int ft) {
  LDS_AS float* cs = (LDS_AS float*)(smem + G1_CSOFF) + PANEL * 32 + 4 * g;
  const f32x4 cg = *(LDS_AS f32x4*)cs, cu = *(LDS_AS f32x4*)(cs + 16);
  float o[2][4];
#pragma unroll
  for (int mf = 0; mf < 2; mf++) {
#pragma unroll
    for (int j = 0; j < 4; j++) {
      float tg = fmaf(-r.mu[mf], cg[j], acc.v[0][mf][j]);
      asm("" : "+v"(tg));
      float tu = fmaf(-r.mu[mf], cu[j], acc.v[1][mf][j]);
      asm("" : "+v"(tu));
      const float e = __builtin_amdgcn_exp2f(tg * r.nrl2[mf]);
      const float q = __builtin_amdgcn_rcpf(fmaf(e, r.sd2[mf], r.sd2[mf]));
      float p = tg * tu;
      asm("" : "+v"(p));
      p *= q;
      asm("" : "+v"(p));
      o[mf][j] = p;
    }
  }
  const auto s0 = __builtin_amdgcn_permlane16_swap(pk2(o[0][0], o[0][1]), pk2(o[1][0], o[1][1]), false, false);
  const auto s1 = __builtin_amdgcn_permlane16_swap(pk2(o[0][2], o[0][3]), pk2(o[1][2], o[1][3]), false, false);
  const u32x4 v = {s0[0], s1[0], s0[1], s1[1]};
  const int ftl = ft & 7;
  const int q = PANEL < 8 ? (PANEL >> 1) * 8 + ftl : 32 + (ftl >> 1);
  const int half = PANEL < 8 ? (PANEL & 1) : (ftl & 1);
  st16_wt(rsh, voff + (u32)((ft >> 3) * 36 + q) * 1024 + (u32)half * 512, v);
}

struct G1Bases { LDS_AS char* b[3]; };
template <int OFF>
DEVI u32x4 g1_ldw(const G1Bases& wb) { constexpr int I = OFF / 49152 < 2 ? OFF / 49152 : 2; return *(LDS_AS u32x4*)(wb.b[I] + (OFF - I * 49152)); }

struct G1Ctx {
  const char *hTw, *Wt, *Wq;
  u32 aoff, loff;
  u32 lds0, ldsw, ldsa;
  u32 rot2k;
  int wid;
  G1Bases wb, ab;
  const char *asrc, *wsrc;
};
template <int I>
DEVI void g2_piece(const G1Ctx& c, int s, u32 slot) {
  if constexpr (I < 4) dma16(c.asrc + (size_t)(I >> 1) * 144 * 1024 + s * 2048 + (I & 1) * 1024, c.loff, c.lds0 + slot + c.wid * 4096 + I * 1024);
  else if constexpr (I < 8) dma16(c.wsrc + (size_t)s * 18432 + (I - 4) * 4096, c.loff, c.ldsw + slot + 16384 + (I - 4) * 4096);
  else { if (c.wid < 2) dma16(c.wsrc + (size_t)s * 18432 + 16384, c.loff, c.ldsw + slot + 32768); }
}
DEVI void g2_stage_dma(const G1Ctx& c, int s, u32 slot) {
  g2_piece<0>(c, s, slot); g2_piece<1>(c, s, slot); g2_piece<2>(c, s, slot); g2_piece<3>(c, s, slot); g2_piece<4>(c, s, slot);
  g2_piece<5>(c, s, slot); g2_piece<6>(c, s, slot); g2_piece<7>(c, s, slot); g2_piece<8>(c, s, slot);
}
DEVI u32 g1_rot(const G1Ctx& c, u32 off) { const u32 x = off + c.rot2k; return x >= 36864u ? x - 36864u : x; }
template <int S, int I>
DEVI void g1_piece(const G1Ctx& c) {
  const u32 po = g1_rot(c, S * 2048);
  constexpr u32 dst = G1_RING + (S % 6) * G1_STAGE;
  if constexpr (I < 2) dma16(c.hTw + I * 16 * (L * 2), c.aoff + po * 64u, c.ldsa + dst + I * 8256);
  else { if ((c.wid >> 2) == (S & 1)) dma16(c.Wq, c.loff + po, c.ldsw + dst + G1_ACT - (S & 1) * 4096); }
}
template <int S> DEVI void g1_stage3(const G1Ctx& c) { g1_piece<S, 0>(c); g1_piece<S, 1>(c); g1_piece<S, 2>(c); }
template <int P>
DEVI void g1_frag(const G1Ctx& c, int fr) {
  fr = fr > 35 ? 35 : fr;
  dma16(c.Wt + P * 36864, c.loff + g1_rot(c, fr * 1024), c.lds0 + g1_region(P) + fr * 1024);
}
template <int PANEL, int I>
DEVI void g1_refill(const G1Ctx& c) {
  if constexpr (PANEL == 3) { if constexpr (I < 2) g1_frag<5>(c, 24 + 8 * I + c.wid); else if constexpr (I < 7) g1_frag<6>(c, 8 * (I - 2) + c.wid); }
  else if constexpr (PANEL < 6) {
    if constexpr (I < 5) g1_frag<PANEL + 3>(c, 8 * I + c.wid);
  }
  else { if (c.wid < 4) g2_piece<I>(c, PANEL - 6, g2_slot(PANEL - 6)); }
}

template <int PANEL, int K>
struct G1PassStep {
  static DEVI void run(G1Acc& acc, u32x4 (&w)[3], const u32x4 (&act)[36], const G1Ctx& c) {
    u32x4 cur = w[K % 3];
    acc.v[K & 1][0] = mfma16(cur, act[K & ~1], acc.v[K & 1][0]);
    acc.v[K & 1][1] = mfma16(cur, act[K | 1], acc.v[K & 1][1]);
    OPAQUE(acc.v[K & 1][0]); OPAQUE(acc.v[K & 1][1]);
    if constexpr (K + 3 < 36) w[K % 3] = g1_ldw<g1_region(PANEL) + (K + 3) * 1024>(c.wb);
    if constexpr (K % 4 == 1) g1_refill<PANEL, K / 4>(c);
    PIN();
    if constexpr (K + 1 < 36) G1PassStep<PANEL, K + 1>::run(acc, w, act, c);
  }
};
template <int PANEL>
DEVI G1Acc g1_pass(const u32x4 (&act)[36], const G1Ctx& c) {
  G1Acc acc = {};
  u32x4 w[3];
  constexpr int R = g1_region(PANEL);
  w[0] = g1_ldw<R>(c.wb); w[1] = g1_ldw<R + 1024>(c.wb); w[2] = g1_ldw<R + 2048>(c.wb);
  PIN();
  G1PassStep<PANEL, 0>::run(acc, w, act, c);
  return acc;
}

template <int J>
DEVI u32x4 g1_lda(const G1Ctx& c) {
  constexpr int OFF = G1_RING + ((J >> 1) % 6) * G1_STAGE + (J & 1) * 32, I = OFF / 49152 < 2 ? OFF / 49152 : 2;
  LDS_AS char* p = c.ab.b[I] + (OFF - I * 49152);
  return tr_read2(p, p + 2048);
}
template <int J, int P>
DEVI u32x4 g1_ldw1(const G1Ctx& c) {
  if constexpr (P == 0) return g1_ldw<RA + J * 1024>(c.wb);
  else return g1_ldw<G1_RING + ((J >> 1) % 6) * G1_STAGE + G1_ACT + (P - 1) * 2048 + (J & 1) * 1024>(c.wb);
}
DEVI void flags_add(const char* fl, int b) { __hip_atomic_fetch_add((u32*)(fl + (b & 15) * 128), 1u, __ATOMIC_RELAXED, __HIP_MEMORY_SCOPE_AGENT); }
DEVI bool flags_set(u32 v) { return __all(v >= 16u); }
DEVI void flags_spin(const char* fl, u32 foff) {
  for (u32 spins = 0; spins < (1u << 22); ++spins) {
    u32 v;
    asm volatile("global_load_dword %0, %1, %2 sc1\n\ts_waitcnt vmcnt(0)" : "=&v"(v) : "v"(foff), "s"(fl) : "memory");
    if (flags_set(v)) break;
    __builtin_amdgcn_s_sleep(1);
  }
}

template <int N> DEVI void waitv() { asm volatile("s_waitcnt vmcnt(%0)" ::"i"(N) : "memory"); }

struct G1Stat {
  const char *stat, *flag, *csp;
  u32 soff, coff, foff; int ft, b;
  u32x2 g[3]; u32 fl; u32x4 cs;
  RtDst rd;
};

template <int J>
struct G1P1Step {
  static DEVI void run(const G1Ctx& c, G1Acc (&acc)[4], u32x4 (&w)[3], u32x4 (&act)[36], G1Stat& st, const u32x4 (&rf)[9], int lane) {
    constexpr int S = J >> 1;
    if constexpr (J & 1) {
      if constexpr (S < 3) { if (c.wid >= 4) waitv<(S == 0 ? 15 : (S == 1 ? 10 : 7))>(); }
      else if constexpr (S <= 8) { if (c.wid >= 4) waitv<6>(); else waitv<9>(); }
      else if constexpr (S <= 13) waitv<6>();
      else if constexpr (S < 17) waitv<S - 4>();
      else waitv<5>();
      BAR();
      if constexpr (S == 11) { if (c.wid == 0 && lane == 0) flags_add(st.flag + FL_RT, st.b); }
      if constexpr (S == 13) {
#pragma unroll
        for (int i = 0; i < 3; i++)
          asm volatile("global_load_dwordx2 %0, %1, %2 sc1" : "=v"(st.g[i]) : "v"(st.soff), "s"(st.stat + i * 2048) : "memory");
        asm volatile("global_load_dwordx4 %0, %1, %2 sc1" : "=v"(st.cs) : "v"(st.coff), "s"(st.csp) : "memory");
      }
      if constexpr (S == 15) asm volatile("global_load_dword %0, %1, %2 sc1" : "=v"(st.fl) : "v"(st.foff), "s"(st.flag + FL_RT) : "memory");
      if constexpr (S == 17) {
        asm volatile("" : "+v"(st.fl));
        if (!flags_set(st.fl)) flags_spin(st.flag + FL_RT, st.foff);
      }
    }
    if constexpr ((J & 1) && J + 1 < 36) { act[J + 1] = g1_lda<J + 1>(c); act[J + 2] = g1_lda<J + 2>(c); }
    PIN();
#define G1_ONE(P)                                              \
    { u32x4 cur = w[P];                                        \
      acc[P].v[J & 1][0] = mfma16(cur, act[J & ~1], acc[P].v[J & 1][0]);   \
      acc[P].v[J & 1][1] = mfma16(cur, act[J | 1], acc[P].v[J & 1][1]);    \
      OPAQUE(acc[P].v[J & 1][0]); OPAQUE(acc[P].v[J & 1][1]);  \
      if constexpr (J + 1 < 36) w[P] = g1_ldw1<J + 1, P>(c);   \
      PIN(); }
    constexpr int TP = 3 + (S - 13) / 2, TF = S == 14 ? 16 : (S == 16 ? 24 : 0);
    constexpr bool T3 = S == 14 || S == 15 || S == 17;
    G1_ONE(0)
    if constexpr ((J & 1) && S > 0) { if constexpr (S < 13) g1_piece<S + 5, 0>(c); else g1_frag<TP>(c, TF + c.wid); PIN(); }
    if constexpr (!(J & 1) && S < 9) {
      if (c.wid < 4) asm volatile("global_store_dwordx4 %0, %1, %2 sc1\n\ts_nop 1" ::"v"(st.rd.voff), "v"(rf[S]), "s"(st.rd.base + rt_off(st.rd, S)) : "memory");
      PIN();
    }
    if constexpr (J == 6) {
      if (st.ft < 18 && st.ft % 3 == 0) {
        float s1[2] = {0.f, 0.f}, s2[2] = {0.f, 0.f};
#pragma unroll
        for (int k = 0; k < 6; k++)
#pragma unroll
          for (int j = 0; j < 4; j++) { s1[k & 1] = dot2(act[k][j], 0x3f803f80u, s1[k & 1]); s2[k & 1] = dot2(act[k][j], act[k][j], s2[k & 1]); }
#pragma unroll
        for (int i = 0; i < 2; i++) {
          s1[i] += __shfl_xor(s1[i], 16); s1[i] += __shfl_xor(s1[i], 32);
          s2[i] += __shfl_xor(s2[i], 16); s2[i] += __shfl_xor(s2[i], 32);
        }
        u32x2 o = {__float_as_uint((lane & 16) ? s1[1] : s1[0]), __float_as_uint((lane & 16) ? s2[1] : s2[0])};
        if (o.y == ~0u) o.y = 0x7fc00000u;
        asm volatile("global_store_dwordx2 %0, %1, %2 sc1\n\ts_nop 1" ::"v"(st.soff & 2047u), "v"(o), "s"(st.stat + (st.ft / 3) * 2048) : "memory");
      }
      PIN();
    }
    G1_ONE(1)
    if constexpr (!(J & 1) && S - 1 > 0 && S - 1 < 13) { g1_piece<S + 4, 2>(c); PIN(); }
    if constexpr ((J & 1) && T3) { g1_frag<TP>(c, TF + 16 + c.wid); PIN(); }
    G1_ONE(2)
    if constexpr ((J & 1) && S > 0) { if constexpr (S < 13) g1_piece<S + 5, 1>(c); else g1_frag<TP>(c, TF + 8 + c.wid); PIN(); }
#undef G1_ONE
    if constexpr (J + 1 < 36) G1P1Step<J + 1>::run(c, acc, w, act, st, rf, lane);
  }
};

template <int SLOT_N, int KK_N, bool SYNC>
DEVI void g2_step(f32x4 (&acc)[2][9], const u32x4 (&A)[2], const u32x4 (&W)[9], u32x4 (&An)[2], u32x4 (&Wn)[9],
                  const G1Bases& ab, const G1Bases& wb) {
#define G2_MM(nf) acc[0][nf] = mfma16(W[nf], A[0], acc[0][nf]); acc[1][nf] = mfma16(W[nf], A[1], acc[1][nf]); \
                  OPAQUE(acc[0][nf]); OPAQUE(acc[1][nf]); PIN();
#define G2_LW(nf) Wn[nf] = g1_ldw<SLOT_N + 16384 + KK_N * 9216 + (nf) * 1024>(wb);
  G2_MM(0)
  An[0] = g1_ldw<SLOT_N + KK_N * 1024>(ab); An[1] = g1_ldw<SLOT_N + 2048 + KK_N * 1024>(ab); PIN();
  G2_MM(1) G2_LW(0) G2_LW(1) PIN();
  G2_MM(2) G2_LW(2) G2_LW(3) PIN();
  G2_MM(3) G2_LW(4) G2_LW(5) PIN();
  G2_MM(4) G2_LW(6) G2_LW(7) PIN();
  G2_MM(5) G2_LW(8) PIN();
  G2_MM(6)
  G2_MM(7)
  if constexpr (SYNC) { WAITL(0); BAR(); PIN(); }
  G2_MM(8)
#undef G2_MM
#undef G2_LW
}
template <int S4, bool LAST>
DEVI void g2_stage(f32x4 (&o)[2][9], u32x4 (&A0)[2], u32x4 (&W0)[9], u32x4 (&A1)[2], u32x4 (&W1)[9], const G1Bases& ab, const G1Bases& wb) {
  g2_step<g2_slot(S4), 1, !LAST>(o, A0, W0, A1, W1, ab, wb);
  g2_step<g2_slot(LAST ? S4 : S4 + 1), 0, false>(o, A1, W1, A0, W0, ab, wb);
}

template <int E0, int NE>
DEVI void cv_copies(LDS_AS char* base, int tt, const u32x4& k0, const u32x4& k1) {
  const u32 w[8] = {k0.x, k0.y, k0.z, k0.w, k1.x, k1.y, k1.z, k1.w};
#pragma unroll
  for (int e = E0; e < E0 + NE; e++) {
    u32x4 o;
#pragma unroll
    for (int j = 0; j < 4; j++)
      o[j] = (e & 1) ? __builtin_amdgcn_alignbyte(w[(e >> 1) + j + 1], w[(e >> 1) + j], 2) : w[(e >> 1) + j];
    *(LDS_AS u32x4*)(base + e * CV_CP + tt * 16) = o;
  }
}
DEVI u32x4 cv_lda(LDS_AS char* p) {
  const u32x2 lo = *(volatile LDS_AS u32x2*)p, hi = *(volatile LDS_AS u32x2*)(p + 8);
  return (u32x4){lo.x, lo.y, hi.x, hi.y};
}
template <int NW, int STRIDE>
DEVI void cv_sum(LDS_AS char* p, f32x4& y0, f32x4& y1) {
  y0 = (f32x4){0, 0, 0, 0}; y1 = y0;
#pragma unroll
  for (int w = 0; w < NW; w++) { y0 += *(LDS_AS f32x4*)(p + w * STRIDE); y1 += *(LDS_AS f32x4*)(p + w * STRIDE + 256); }
}
DEVI u32x4 cv_out(const f32x4& y0, const f32x4& y1, const u32x4& uv, const u32x4& xv, float dsk) {
  const float y[8] = {y0[0], y0[1], y0[2], y0[3], y1[0], y1[1], y1[2], y1[3]};
  u32x4 o;
#pragma unroll
  for (int j = 0; j < 4; j++) {
    const u32 yb = pk2(y[2 * j], y[2 * j + 1]), sk = pk2(bflo(uv[j]) * dsk, bfhi(uv[j]) * dsk);
    const u32 z = pk2(bflo(yb) + bflo(sk), bfhi(yb) + bfhi(sk));
    o[j] = pk2(bflo(xv[j]) + bflo(z), bfhi(xv[j]) + bfhi(z));
  }
  return o;
}

__global__ __launch_bounds__(512, 2) void k2_main(K2Args a) {
  extern __shared__ __attribute__((aligned(16))) char smem_[];
  LDS_AS char* smem = (LDS_AS char*)smem_;
  const int tid = threadIdx.x, lane = tid & 63;
  const int wid = __builtin_amdgcn_readfirstlane(tid >> 6);
  const int b = blockIdx.x, xcd = b & 7, rr = b >> 3;
  const int mt = (xcd & 1) * 4 + (rr & 3), ft = (xcd >> 1) * 8 + (rr >> 2);
  TS_DECL; TS(0);
  G1Ctx c;
  c.wid = wid;
  c.rot2k = (ft < 18 ? ft : ft - 18) * 2048;
  c.lds0 = (u32)(size_t)smem;
  c.ldsw = c.lds0 + wid * 1024;
  c.ldsa = c.ldsw + (wid >> 2) * 32;
  c.hTw = a.ws + WS_HT + mt * 512 + (size_t)(2 * wid) * (L * 2);
  c.Wt = a.ws + WS_WT + (size_t)ft * 9 * 36864;
  c.Wq = c.Wt + (1 + ((wid >> 1) & 1)) * 36864 + (wid & 1) * 1024;
  c.loff = lane * 16;
  const int arow = lane >> 5;
  c.aoff = arow * (L * 2) + (((lane & 31) ^ (((2 * wid + arow) & 3) << 2)) * 16);
  const int unit = b < 128 ? ((((b >> 4) * 9 + 2 * ((b >> 2) & 3) + 1)) << 2) + (b & 3) : 288 + (b - 128);
  u32x4 rv[5], rf[9];
  RtDst rd;
  {
    const int half = wid >> 2, wl = wid & 3, tt = tid & 255;
    const int dm = b + 256 * half;
    const int dc = 512 + (b >> 2), nlo = b & 3, nhi = 7 - nlo;
    const u32x4* krm = (const u32x4*)(a.ws + WS_KREV + (size_t)dm * KREV_PITCH);
    const u32x4* krc = (const u32x4*)(a.ws + WS_KREV + (size_t)dc * KREV_PITCH);
    const char* ux = a.ws + WS_UX + (size_t)(tt >> 2) * D * 128 + (tt & 3) * 16;
    const u32x4 k0 = krm[tt], k1 = krm[tt + 1], uv = *(const u32x4*)(ux + dm * 128);
    const u32x4 kc0 = krc[tt], kc1 = krc[tt + 1], uc = *(const u32x4*)(ux + dc * 128);
    const u32x4 xv = *(const u32x4*)(ux + dm * 128 + 64);
    const int te = ((tt & 32) ? nlo : nhi) * 32 + (tt & 31);
    const char* uxe = a.ws + WS_UX + ((size_t)(te >> 2) * D + dc) * 128 + (te & 3) * 16;
    const u32x4 ue = *(const u32x4*)uxe, xe = *(const u32x4*)(uxe + 64);
    const float dskm = bf2f(a.dskip[dm]), dskc = bf2f(a.dskip[dc]);
    const u32x4* rp[5];
    retile_addr<512, 5>(a.w1, a.w3, a.w2, unit, tid, rp);
#pragma unroll
    for (int i = 0; i < 5; i++) OPAQUE(rp[i]);
    LDS_AS char* cm = smem + CV_K0 + half * CV_BYTES;
    LDS_AS char* cc = smem + CV_K0 + 2 * CV_BYTES;
    cv_copies<0, 4>(cm, tt, k0, k1);
    *(LDS_AS u32x4*)(cm + CV_UOFF + 512 + tt * 16) = uv;
    if (half == 0) cv_copies<0, 2>(cc, tt, kc0, kc1);
    else { cv_copies<2, 2>(cc, tt, kc0, kc1); *(LDS_AS u32x4*)(cc + CV_UOFF + 512 + tt * 16) = uc; }
    {
      const u32x4 z = {0, 0, 0, 0};
      if (tid == 511) *(LDS_AS u32*)(smem + G1_CSOFF) = 0u;
      if (tid < 144) {
        const int ch = tid / 48, i = tid - ch * 48;
        *(LDS_AS u32x4*)(smem + CV_K0 + ch * CV_BYTES + (i < 16 ? (i >> 2) * CV_CP + 4096 + (i & 3) * 16 : CV_UOFF + (i - 16) * 16)) = z;
      }
    }
    __builtin_amdgcn_s_waitcnt(0x0070);
    BAR();
    TS(1);
    const int i16 = lane & 15, g = lane >> 4;
    const int aoff = ((3 - i16) & 3) * CV_CP + (2028 - (i16 & 12) + 8 * g) * 2;
    const int boff = CV_UOFF + (2 * i16 + g + 30) * 16;
    f32x4 acc[8], eh = {0, 0, 0, 0}, el = eh;
#pragma unroll
    for (int n = 0; n < 8; n++) acc[n] = (f32x4){0, 0, 0, 0};
    {
      LDS_AS char* ap = cm + aoff - 128 * wl;
      LDS_AS char* bp = cm + boff - 128 * wl;
      LDS_AS char* apc = cc + aoff - 64 * wid;
      LDS_AS char* bh = cc + boff - 64 * wid + 512 * nhi;
      LDS_AS char* bl = cc + boff - 64 * wid + 512 * nlo;
      u32x4 A[2][8], B[2][8], Ac[8], Bh[8], Bl[4];
      A[0][0] = cv_lda(ap);
#pragma unroll
      for (int j = 0; j < 8; j++) B[0][j] = *(LDS_AS u32x4*)(bp + 512 * j);
      A[0][1] = cv_lda(ap - 512);
      PIN();
#pragma unroll
      for (int a1 = 0; a1 < 8; a1++) {
        if (a1 + 2 < 8) A[0][a1 + 2] = cv_lda(ap - 512 * (a1 + 2));
        B[1][a1] = *(LDS_AS u32x4*)(bp - 64 + 512 * a1);
        if (a1 >= 6) A[1][a1 - 6] = cv_lda(ap - 64 - 512 * (a1 - 6));
        PIN();
#pragma unroll
        for (int n = a1; n < 8; n++) acc[n] = mfma16(A[0][a1], B[0][n - a1], acc[n]);
        if (a1 < 5) rv[a1] = __builtin_nontemporal_load(rp[a1]);
        PIN();
      }
#pragma unroll
      for (int a1 = 0; a1 < 8; a1++) {
        if (a1 + 2 < 8) A[1][a1 + 2] = cv_lda(ap - 64 - 512 * (a1 + 2));
        Ac[a1] = cv_lda(apc - 512 * a1);
        Bh[a1] = *(LDS_AS u32x4*)(bh - 512 * (a1 <= nhi ? a1 : nhi));
        if (a1 < 4) Bl[a1] = *(LDS_AS u32x4*)(bl - 512 * (a1 <= nlo ? a1 : nlo));
        PIN();
#pragma unroll
        for (int n = a1; n < 8; n++) acc[n] = mfma16(A[1][a1], B[1][n - a1], acc[n]);
        PIN();
      }
#pragma unroll
      for (int a1 = 0; a1 < 8; a1++) {
        if (a1 <= nhi) eh = mfma16(Ac[a1], Bh[a1], eh);
        if (a1 < 4 && a1 <= nlo) el = mfma16(Ac[a1], Bl[a1], el);
      }
    }
    BAR();
    LDS_AS char* pm = smem + CV_K0 + half * 32768;
    LDS_AS char* pc = smem + CV_K0 + 65536;
#pragma unroll
    for (int n = 0; n < 8; n++) *(LDS_AS f32x4*)(pm + wl * 8192 + n * 1024 + lane * 16) = acc[n];
    *(LDS_AS f32x4*)(pc + wid * 2048 + lane * 16) = eh;
    *(LDS_AS f32x4*)(pc + wid * 2048 + 1024 + lane * 16) = el;
    WAITL(0); BAR();
    {
      const int soff = ((tt >> 5) * 64 + ((tt & 31) >> 1) + 32 * (tt & 1)) * 16;
      f32x4 y0, y1;
      cv_sum<4, 8192>(pm + soff, y0, y1);
      st16_wt(mkrsrc(a.ws + WS_HT + (size_t)dm * (L * 2), L * 2), tt * 16, cv_out(y0, y1, uv, xv, dskm));
      if (wid == 4) {
        cv_sum<8, 2048>(pc + soff, y0, y1);
        st16_wt(mkrsrc(a.ws + WS_HT + (size_t)dc * (L * 2), L * 2), te * 16, cv_out(y0, y1, ue, xe, dskc));
      }
    }
    retile_stage<512, 5>(smem + G1_RT, unit, tid, rv);
    WAITV(0);
    __builtin_amdgcn_s_waitcnt(0x0070);
    BAR();
    TS(2);
    if (tid == 0) flags_add(a.ws + WS_FLAG + FL_A, b);
    rd = retile_read(a.ws, smem + G1_RT, unit, lane, wl, wid < 4, rf);
    WAITL(0);
    if (wid >= 4) {
      volatile LDS_AS u32* go = (volatile LDS_AS u32*)(smem + G1_CSOFF);
      if (wid == 4) { flags_spin(a.ws + WS_FLAG + FL_A, (lane & 15) * 128); *go = 1u; }
      else {
#pragma unroll 1
        for (int i = wid - 5; i < 36; i += 3) g1_frag<0>(c, i);
        for (u32 spins = 0; *go == 0u && spins < (1u << 24); ++spins) __builtin_amdgcn_s_sleep(1);
      }
      TS(3);
#pragma unroll 1
      for (int S = 0; S < 4; S++) {
        const u32 po = g1_rot(c, S * 2048), dst = c.ldsw - 4096 + G1_RING + S * G1_STAGE;
#pragma unroll
        for (int i = 0; i < 4; i++) dma16(c.hTw + (i - 1) * 8 * (L * 2), c.aoff + po * 64u, dst + i * 4128);
        dma16(c.Wq, c.loff + po, dst + G1_ACT);
      }
      WAITV(15);
    }
  }
  const int hh = lane >> 5, g4 = lane >> 4, q4 = (lane & 15) >> 2, p4 = lane & 3;
#pragma unroll
  for (int i = 0; i < 3; i++) {
    c.ab.b[i] = smem + (8 * g4 + q4) * 512 + g4 * 32 + ((wid ^ q4) * 64) + p4 * 8 + i * 49152;
    c.wb.b[i] = smem + lane * 16 + i * 49152;
    OPAQUE(c.ab.b[i]); OPAQUE(c.wb.b[i]);
  }
  G1Stat st;
  st.stat = a.ws + WS_STAT + (size_t)mt * 18 * 2048;
  st.flag = a.ws + WS_FLAG;
  st.csp = a.ws + WS_CSP + ft * 8192;
  st.soff = hh * 3 * 2048 + (wid * 32 + (lane & 31)) * 8;
  st.coff = tid * 16;
  st.foff = (lane & 15) * 128;
  st.ft = ft; st.b = b;
  u32x4 act[36], w[3];
  G1Acc acc[4] = {};
  BAR();
  TS(4);
  g1_stage3<4>(c); g1_stage3<5>(c);
  act[0] = g1_lda<0>(c); act[1] = g1_lda<1>(c);
  w[0] = g1_ldw1<0, 0>(c); w[1] = g1_ldw1<0, 1>(c); w[2] = g1_ldw1<0, 2>(c);
  PIN();
  st.rd = rd;
  G1P1Step<0>::run(c, acc, w, act, st, rf, lane);
  TS(5);
  asm volatile("" : "+v"(st.g[0]), "+v"(st.g[1]), "+v"(st.g[2]), "+v"(st.cs));
  *(LDS_AS float*)(smem + G1_CSOFF + tid * 4) = (__uint_as_float(st.cs[0]) + __uint_as_float(st.cs[1])) + (__uint_as_float(st.cs[2]) + __uint_as_float(st.cs[3]));
  {
    bool bad = st.g[0].y == ~0u || st.g[1].y == ~0u || st.g[2].y == ~0u;
    for (u32 spins = 0; __any(bad) && spins < (1u << 22); ++spins) {
      __builtin_amdgcn_s_sleep(2);
      bad = false;
#pragma unroll
      for (int i = 0; i < 3; i++) {
        asm volatile("global_load_dwordx2 %0, %1, %2 sc1\n\ts_waitcnt vmcnt(0)" : "=&v"(st.g[i]) : "v"(st.soff), "s"(st.stat + i * 2048) : "memory");
        bad |= (st.g[i].y == ~0u);
      }
    }
  }
  float s1 = 0.f, s2 = 0.f;
#pragma unroll
  for (int i = 0; i < 3; i++) { s1 += __uint_as_float(st.g[i].x); s2 += __uint_as_float(st.g[i].y); }
  s1 += __shfl_xor(s1, 32); s2 += __shfl_xor(s2, 32);
  const float mean = s1 * (1.0f / D);
  const float var = fmaxf(s2 * (1.0f / D) - mean * mean, 0.f);
  const float mu = rbf(mean);
  const float sd = rbf(sqrtf(rbf(rbf(var) + 1e-5f)));
  const float mu2 = __shfl_xor(mu, 16), sdo = __shfl_xor(sd, 16);
  G1Row row;
  row.mu[0] = (lane & 16) ? mu2 : mu; row.mu[1] = (lane & 16) ? mu : mu2;
  const float sdA = (lane & 16) ? sdo : sd, sdB = (lane & 16) ? sd : sdo;
  row.sd2[0] = sdA * sdA; row.sd2[1] = sdB * sdB;
  row.nrl2[0] = -1.44269504088896341f / sdA; row.nrl2[1] = -1.44269504088896341f / sdB;
  auto rsh = mkrsrc(a.ws + WS_HFF, (u32)HFF_BYTES);
  const u32 voff = (u32)(mt * 16 + wid * 2 + (g4 & 1)) * (144 * 1024) + (u32)((lane & 15) + 16 * hh) * 16;

  const int ks = xcd >> 1, mt16 = (xcd & 1) * 8 + (rr & 7), nt = rr >> 3;
  const int tile = mt16 * 4 + nt;
  c.asrc = a.ws + WS_HFF + ((size_t)(mt16 * 8 + 2 * (wid & 3)) * 144 + ks * 36) * 1024;
  c.wsrc = a.ws + WS_W2T + ((size_t)nt * 144 + ks * 36) * 9216 + (wid & 3) * 1024;
  u32* fl_out = (u32*)(a.ws + WS_FLAG + FL_HFF + (size_t)(((mt * 2 + (wid >> 2)) * 4 + (ft >> 3)) * 3) * 128);
  const char* fl_in = a.ws + WS_FLAG + FL_HFF + (size_t)((mt16 * 4 + ks) * 3) * 128;
  auto publish = [&](int f) { if (lane == 0) __hip_atomic_fetch_add(fl_out + f * 32, 1u, __ATOMIC_RELAXED, __HIP_MEMORY_SCOPE_AGENT); };
  u32 pollv;
  auto poll_issue = [&](int f) { asm volatile("global_load_dword %0, %1, %2 sc1" : "=v"(pollv) : "v"(0u), "s"(fl_in + f * 128) : "memory"); };
  auto poll_check = [&](int f) {
    asm volatile("" : "+v"(pollv));
    for (u32 spins = 0; __builtin_amdgcn_readfirstlane(pollv) < 32u && spins < (1u << 22); ++spins) {
      __builtin_amdgcn_s_sleep(2);
      asm volatile("global_load_dword %0, %1, %2 sc1\n\ts_waitcnt vmcnt(0)" : "=&v"(pollv) : "v"(0u), "s"(fl_in + f * 128) : "memory");
    }
  };

#define EPI(P, A) g1_epilogue<P>(A, smem, g4, row, rsh, voff, ft); PIN()
  const bool early = wid >= 4;
  WAITL(0); BAR();
  asm volatile("global_load_dwordx4 %0, %1, %2 sc1" : "=v"(st.cs) : "v"(st.coff), "s"(st.csp) : "memory");
  if (early) { EPI(0, acc[0]); EPI(1, acc[1]); }
  acc[3] = g1_pass<3>(act, c);
  if (!early) { EPI(0, acc[0]); EPI(1, acc[1]); }
  WAITV(9);
  asm volatile("" : "+v"(st.cs));
  if (tid >= 160) *(LDS_AS float*)(smem + G1_CSOFF + tid * 4) = (__uint_as_float(st.cs[0]) + __uint_as_float(st.cs[1])) + (__uint_as_float(st.cs[2]) + __uint_as_float(st.cs[3]));
  WAITL(0); BAR();
  if (early) { EPI(2, acc[2]); EPI(3, acc[3]); }
  acc[0] = g1_pass<4>(act, c);
  if (!early) { EPI(2, acc[2]); EPI(3, acc[3]); }
  TS(6);
  WAITV(7);
  publish(0);
  BAR();
  if (early) { EPI(4, acc[0]); }
  acc[1] = g1_pass<5>(act, c);
  if (!early) { poll_issue(0); EPI(4, acc[0]); EPI(5, acc[1]); WAITV(2); poll_check(0); }
  else WAITV(7);
  BAR();
  if (early) { EPI(5, acc[1]); }
  acc[2] = g1_pass<6>(act, c);
  if (!early) { EPI(6, acc[2]); } else WAITV(1);
  BAR();
  if (early) { EPI(6, acc[2]); }
  acc[3] = g1_pass<7>(act, c);
  if (!early) { EPI(7, acc[3]); WAITV(9); } else WAITV(1);
  publish(1);
  TS(7);
  BAR();
  if (early) { EPI(7, acc[3]); }
  acc[0] = g1_pass<8>(act, c);
  EPI(8, acc[0]);
  TS(8);
#undef EPI
  BAR();

  u32* cnt_in = (u32*)(a.ws + WS_FLAG + (size_t)(tile * 4 + ks) * 128);
  if (!early) {
    poll_issue(1);
    g2_stage_dma(c, 3, g2_slot(3));
    {
      const char* hsrc = a.ws + WS_HT + (size_t)(nt * 144) * (L * 2) + (mt16 * 128 + ks * 32) * 2;
      const u32 hoff = (lane >> 2) * (L * 2) + (lane & 3) * 16;
#pragma unroll
      for (int i = 0; i < 3; i++) { const int pc = (wid + 4 * i < 9) ? wid + 4 * i : wid; dma16(hsrc + (size_t)pc * 16 * (L * 2), hoff, c.lds0 + G2_RES + pc * 1024); }
    }
    WAITV(11);
    poll_check(1);
    publish(2);
    BAR();
    g2_stage_dma(c, 4, g2_slot(0));
    BAR();
    g2_stage_dma(c, 5, g2_slot(1));
#define G2_LD(s) { if (wid < 2) WAITV(18); else WAITV(16); BAR(); g2_stage_dma(c, (s) + 4, g2_slot(s)); }
    G2_LD(2) G2_LD(3) G2_LD(4)
    poll_issue(2);
    G2_LD(5) G2_LD(6) G2_LD(7)
    poll_check(2);
    G2_LD(8) G2_LD(9) G2_LD(10) G2_LD(11) G2_LD(12) G2_LD(13)
#undef G2_LD
    if (wid < 2) WAITV(18); else WAITV(16);
    BAR();
    WAITV(8); BAR();
    WAITV(0); BAR();
  } else {
    const int mw = wid - 4;
    G1Bases ab;
#pragma unroll
    for (int i = 0; i < 3; i++) { ab.b[i] = smem + mw * 4096 + lane * 16 + i * 49152; OPAQUE(ab.b[i]); }
    f32x4 o[2][9];
#pragma unroll
    for (int i = 0; i < 2; i++)
#pragma unroll
      for (int j = 0; j < 9; j++) { o[i][j] = (f32x4){0, 0, 0, 0}; OPAQUE(o[i][j]); }
    u32x4 A0[2], W0[9], A1[2], W1[9];
    constexpr int S0 = g2_slot(0);
    A0[0] = g1_ldw<S0>(ab); A0[1] = g1_ldw<S0 + 2048>(ab);
    W0[0] = g1_ldw<S0 + 16384>(c.wb); W0[1] = g1_ldw<S0 + 17408>(c.wb); W0[2] = g1_ldw<S0 + 18432>(c.wb); W0[3] = g1_ldw<S0 + 19456>(c.wb);
    W0[4] = g1_ldw<S0 + 20480>(c.wb); W0[5] = g1_ldw<S0 + 21504>(c.wb); W0[6] = g1_ldw<S0 + 22528>(c.wb); W0[7] = g1_ldw<S0 + 23552>(c.wb);
    W0[8] = g1_ldw<S0 + 24576>(c.wb);
    PIN();
#pragma unroll 1
    for (int it = 0; it < 4; it++) {
      g2_stage<0, false>(o, A0, W0, A1, W1, ab, c.wb);
      g2_stage<1, false>(o, A0, W0, A1, W1, ab, c.wb);
      if (it == 0) { WAITV(0); publish(2); }
      g2_stage<2, false>(o, A0, W0, A1, W1, ab, c.wb);
      g2_stage<3, false>(o, A0, W0, A1, W1, ab, c.wb);
    }
    g2_stage<0, false>(o, A0, W0, A1, W1, ab, c.wb);
    g2_stage<1, true>(o, A0, W0, A1, W1, ab, c.wb);
    TS(9);
    if (mw != ks) {
      const auto rs_out = mkrsrc(a.ws + WS_PART + ((size_t)(tile * 4 + mw) * 4 + ks) * 9216, 9216);
#pragma unroll
      for (int j = 0; j < 9; j++) {
        u32x4 v = {pk2(o[0][j][0], o[0][j][1]), pk2(o[0][j][2], o[0][j][3]), pk2(o[1][j][0], o[1][j][1]), pk2(o[1][j][2], o[1][j][3])};
        st16_wt(rs_out, (j * 64 + lane) * 16, v);
      }
      WAITV(0);
      if (lane == 0) __hip_atomic_fetch_add((u32*)(a.ws + WS_FLAG + (size_t)(tile * 4 + mw) * 128), 1u, __ATOMIC_RELAXED, __HIP_MEMORY_SCOPE_AGENT);
    } else {
#pragma unroll
      for (int j = 0; j < 9; j++) {
        *(LDS_AS f32x4*)(smem + g2_slot(2) + ((j * 2) * 64 + lane) * 16) = o[0][j];
        *(LDS_AS f32x4*)(smem + g2_slot(2) + ((j * 2 + 1) * 64 + lane) * 16) = o[1][j];
      }
      WAITL(0);
    }
  }
  BAR();
  {
    u32 spins = 0;
    while (__builtin_amdgcn_readfirstlane(__hip_atomic_load(cnt_in, __ATOMIC_RELAXED, __HIP_MEMORY_SCOPE_AGENT)) < 3u) {
      __builtin_amdgcn_s_sleep(1);
      if (++spins > (1u << 24)) break;
    }
    __builtin_amdgcn_fence(__ATOMIC_ACQUIRE, "wavefront");
    TS(10);
    const auto rs_in = mkrsrc(a.ws + WS_PART + (size_t)(tile * 4 + ks) * 4 * 9216, 4 * 9216);
    int ln = threadIdx.x; asm volatile("" : "+v"(ln)); ln &= 63;
    const int lane = ln, g = ln >> 4, q4 = (ln & 15) >> 2, p4 = ln & 3;
    for (int j = wid; j < 9; j += 8) {
      u32x4 pp[3];
#pragma unroll
      for (int sidx = 0; sidx < 3; sidx++)
        pp[sidx] = __builtin_amdgcn_raw_buffer_load_b128(rs_in, ((ks + 1 + sidx) & 3) * 9216 + (j * 64 + lane) * 16, 0, 16);
      LDS_AS char* rb = smem + G2_RES + (j * 16 + 4 * g + q4) * 64 + p4 * 8;
      u16* orow = a.out + (size_t)(mt16 * 128 + ks * 32 + (lane & 15)) * D + nt * 144 + j * 16 + 4 * g;
#pragma unroll
      for (int i = 0; i < 2; i++) {
        const u32x2 hres = __builtin_bit_cast(u32x2, tr_read(rb + i * 32));
        f32x4 v = *(LDS_AS f32x4*)(smem + g2_slot(2) + ((j * 2 + i) * 64 + lane) * 16);
#pragma unroll
        for (int sidx = 0; sidx < 3; sidx++) {
          v[0] += bflo(pp[sidx][2 * i]); v[1] += bfhi(pp[sidx][2 * i]);
          v[2] += bflo(pp[sidx][2 * i + 1]); v[3] += bfhi(pp[sidx][2 * i + 1]);
        }
        const u32 o01 = pk2(v[0], v[1]), o23 = pk2(v[2], v[3]);
        u32x2 ov = {pk2(bflo(hres.x) + bflo(o01), bfhi(hres.x) + bfhi(o01)), pk2(bflo(hres.y) + bflo(o23), bfhi(hres.y) + bfhi(o23))};
        *(u32x2*)(orow + (size_t)i * 16 * D) = ov;
      }
    }
  }
  TS(11);
  TS_OUT(1);
}

extern "C" void kernel_launch(void* const* d_in, const int* in_sizes, int n_in, void* d_out, int out_size, void* d_ws,
                              size_t ws_size, hipStream_t stream) {
  (void)in_sizes; (void)n_in; (void)out_size; (void)ws_size;
  (void)hipFuncSetAttribute((const void*)k2_main, hipFuncAttributeMaxDynamicSharedMemorySize, K2_LDS);
  K1Args a1;
  a1.x = (const u16*)d_in[0]; a1.Are = (const u16*)d_in[1]; a1.Aim = (const u16*)d_in[2];
  a1.Bre = (const u16*)d_in[3]; a1.Bim = (const u16*)d_in[4]; a1.Cre = (const u16*)d_in[5];
  a1.Cim = (const u16*)d_in[6]; a1.ldt = (const u16*)d_in[7]; a1.w1 = (const u16*)d_in[10];
  a1.w3 = (const u16*)d_in[11]; a1.w2 = (const u16*)d_in[12]; a1.ws = (char*)d_ws;
  k1_pre<<<K1_LN + K1_KG + K1_PT + (K1ORD == 2 ? 64 : 0), 256, K1_LDS, stream>>>(a1);
  K2Args a2;
  a2.dskip = (const u16*)d_in[8]; a2.w1 = (const u16*)d_in[10]; a2.w3 = (const u16*)d_in[11]; a2.w2 = (const u16*)d_in[12];
  a2.ws = (char*)d_ws; a2.out = (u16*)d_out;
  k2_main<<<256, 512, K2_LDS, stream>>>(a2);
}
```

```cpp
#define DUMP_TS
#define PROBE 0
#include <hip/hip_runtime.h>
#include <stdint.h>

typedef unsigned short u16;
typedef unsigned int u32;
typedef unsigned long long u64;
typedef __bf16 bf2 __attribute__((ext_vector_type(2)));
typedef __bf16 bf8 __attribute__((ext_vector_type(8)));
typedef short s4 __attribute__((ext_vector_type(4)));
typedef u32 u32x2 __attribute__((ext_vector_type(2)));
typedef u32 u32x4 __attribute__((ext_vector_type(4)));
typedef float f32x2 __attribute__((ext_vector_type(2)));
typedef float f32x4 __attribute__((ext_vector_type(4)));
typedef float f32x16 __attribute__((ext_vector_type(16)));
#define LDS_AS __attribute__((address_space(3)))
#define DEVI __device__ __forceinline__

#define NT_X
#define NT_P
constexpr int L = 2048, D = 576, NM = 128, F = 4608;

constexpr size_t WS_UX   = 0;
constexpr size_t WS_HT   = WS_UX + (size_t)D * L * 4;
constexpr int    KREV_PITCH = 4352;
constexpr size_t WS_KREV = WS_HT + (size_t)D * L * 2;
constexpr size_t WS_WT   = WS_KREV + (size_t)D * KREV_PITCH;
constexpr size_t WT_BYTES = (size_t)288 * 36 * 1024;
constexpr size_t WS_W2T  = WS_WT + WT_BYTES + 65536;
constexpr size_t W2T_BYTES = (size_t)4 * 144 * 9 * 1024;
constexpr size_t WS_CSP  = WS_W2T + W2T_BYTES + 65536;
constexpr size_t WS_HFF  = WS_CSP + (size_t)32 * 4 * 512 * 4;
constexpr size_t HFF_BYTES = (size_t)128 * 144 * 1024;
constexpr size_t WS_PART = WS_HFF + HFF_BYTES;
constexpr size_t PART_BYTES = (size_t)64 * 4 * 4 * 9216;
constexpr size_t WS_FLAG = WS_PART + PART_BYTES;
constexpr size_t FLAG_BYTES = (size_t)512 * 128;
constexpr size_t WS_STAT = WS_FLAG + FLAG_BYTES;
constexpr size_t STAT_BYTES = (size_t)8 * 18 * 256 * 8;
constexpr size_t WS_SCR = WS_STAT + STAT_BYTES;

DEVI float bf2f(u32 v) { return __uint_as_float(v << 16); }
DEVI float bflo(u32 v) { return __uint_as_float(v << 16); }
DEVI float bfhi(u32 v) { return __uint_as_float(v & 0xffff0000u); }
DEVI u32 pk2(float lo, float hi) {
  bf2 r = {(__bf16)lo, (__bf16)hi};
  return __builtin_bit_cast(u32, r);
}
DEVI float rbf(float x) { return __uint_as_float(((u32)__builtin_bit_cast(u16, (__bf16)x)) << 16); }
DEVI void dma16(const char* sbase, u32 voff, u32 lds) {
  asm volatile("s_mov_b32 m0, %2\n\ts_nop 0\n\tglobal_load_lds_dwordx4 %0, %1 sc1" ::"v"(voff), "s"(sbase), "s"(lds) : "memory");
}
#define OPAQUE(x) asm volatile("" : "+v"(x))
#define PIN() __builtin_amdgcn_sched_barrier(0)
DEVI s4 tr_read(LDS_AS void* p) { return __builtin_amdgcn_ds_read_tr16_b64_v4i16((LDS_AS s4*)p); }
DEVI u32x4 tr_read2(LDS_AS char* p0, LDS_AS char* p1) {
  u32x2 a = __builtin_bit_cast(u32x2, tr_read(p0)), b = __builtin_bit_cast(u32x2, tr_read(p1));
  u32x4 r = {a.x, a.y, b.x, b.y};
  return r;
}
DEVI void st16_wt(__amdgpu_buffer_rsrc_t rs, u32 off, u32x4 v) { __builtin_amdgcn_raw_buffer_store_b128(v, rs, off, 0, 16); }
DEVI __amdgpu_buffer_rsrc_t mkrsrc(const void* p, u32 bytes) {
  return __builtin_amdgcn_make_buffer_rsrc((void*)p, 0, bytes, 0x00020000);
}
#define WAITV(n) asm volatile("s_waitcnt vmcnt(" #n ")" ::: "memory")
#define WAITL(n) asm volatile("s_waitcnt lgkmcnt(" #n ")" ::: "memory")
#define BAR() __builtin_amdgcn_s_barrier()
DEVI f32x16 mfma32(u32x4 a, u32x4 b, f32x16 c) {
  return __builtin_amdgcn_mfma_f32_32x32x16_bf16(__builtin_bit_cast(bf8, a), __builtin_bit_cast(bf8, b), c, 0, 0, 0);
}
DEVI f32x4 mfma16(u32x4 a, u32x4 b, f32x4 c) {
  return __builtin_amdgcn_mfma_f32_16x16x32_bf16(__builtin_bit_cast(bf8, a), __builtin_bit_cast(bf8, b), c, 0, 0, 0);
}
typedef _Float16 h2v __attribute__((ext_vector_type(2)));
typedef _Float16 h8v __attribute__((ext_vector_type(8)));
DEVI u32 pkh2(float lo, float hi) { h2v r = {(_Float16)lo, (_Float16)hi}; return __builtin_bit_cast(u32, r); }
DEVI f32x4 mfma16h(u32x4 a, u32x4 b, f32x4 c) {
  return __builtin_amdgcn_mfma_f32_16x16x32_f16(__builtin_bit_cast(h8v, a), __builtin_bit_cast(h8v, b), c, 0, 0, 0);
}
template <int CTRL> DEVI float dpp(float v) {
  return __builtin_bit_cast(float, __builtin_amdgcn_update_dpp(0, __builtin_bit_cast(int, v), CTRL, 0xf, 0xf, true));
}
DEVI float sum8(float v) {
  v += dpp<0xB1>(v); v += dpp<0x4E>(v); v += dpp<0x141>(v);
  return v;
}
DEVI float dot2(u32 a, u32 b, float c) {
  return __builtin_amdgcn_fdot2_f32_bf16(__builtin_bit_cast(bf2, a), __builtin_bit_cast(bf2, b), c, false);
}

constexpr size_t WS_TS = (size_t)200 << 20;
#define TS_DECL u32 ts_r[12] = {}, ts_c[12] = {}
#define TS(idx) do { __builtin_amdgcn_sched_barrier(0); u64 r_, c_; asm volatile("s_memrealtime %0\n\ts_memtime %1\n\ts_waitcnt lgkmcnt(0)" : "=&s"(r_), "=&s"(c_) :: "memory"); \
    ts_r[idx] = (u32)r_; ts_c[idx] = (u32)c_; __builtin_amdgcn_sched_barrier(0); } while (0)
#define TS_OUT(kern) do { if ((threadIdx.x & 63) == 0) { u32* tsp = (u32*)(a.ws + WS_TS) + (((size_t)(kern) * 1024 + b) * 8 + (threadIdx.x >> 6)) * 32; \
    for (int i_ = 0; i_ < 12; i_++) { tsp[2 * i_] = ts_r[i_]; tsp[2 * i_ + 1] = ts_c[i_]; } \
    tsp[24] = __builtin_amdgcn_s_getreg(4 | (31 << 11)); tsp[25] = __builtin_amdgcn_s_getreg(20 | (31 << 11)); } } while (0)

#define K1ORD 1
constexpr int K1_KG = 576, K1_LN = 128, K1_PT = 176;
constexpr int K1_LDS = 38 * 1024;

struct K1Args {
  const u16 *x, *Are, *Aim, *Bre, *Bim, *Cre, *Cim, *ldt, *w1, *w3, *w2;
  char* ws;
};

constexpr int lag_index(int l) {
  if (l < 256) return l;
  int e = 0;
  while ((l >> e) >= 256) e++;
  int q = l >> e;
  const int rem = l & ((1 << e) - 1), half = 1 << (e - 1);
  if (rem > half || (rem == half && (q & 1))) q++;
  return 128 * e + q;
}
constexpr int kg_p1(int r) { return r < 8 ? 256 + 2 * r : 512 + 256 * ((r - 8) >> 2) + 4 * ((r - 8) & 3); }
constexpr int kg_p2(int r) { return r < 8 ? 1024 + 256 * (r >> 1) + 8 * (r & 1) : 2048; }
struct KgTab {
  u16 g[256][8]; u16 t[256][2];
  constexpr KgTab() : g(), t() {
    for (int i = 0; i < 256; i++) {
      for (int j = 0; j < 8; j++) g[i][j] = (u16)(4 * lag_index(2047 - (i * 8 + j)));
      const int r = i & 15, c16 = (i >> 4) * 16;
      t[i][0] = (u16)(4 * lag_index(kg_p1(r) + c16));
      t[i][1] = (u16)(4 * ((r < 8 || i == 8) ? lag_index(kg_p2(r) + c16) : 700));
    }
  }
};
__constant__ __attribute__((aligned(16))) const KgTab kg_tab{};

DEVI int panel_log(int ft, int p) { const int ls = 4 - (ft & 3); return p < ls ? p : (p < ls + 4 ? p - ls + 5 : p - 4); }
DEVI int panel_phys(int ft, int lp) { const int ls = 4 - (ft & 3); return lp < ls ? lp : (lp < 5 ? lp + 4 : lp - 5 + ls); }

template <int NT, int NV>
DEVI void retile_addr(const u16* w1, const u16* w3, const u16* w2, int unit, int tid, const u32x4* (&p)[NV]) {
  if (unit < 288) {
    const int fq = unit >> 2, kq = unit & 3, ch = tid & 15;
    const u16* src = ((ch >> 3) ? w3 : w1) + (size_t)(kq * 144) * F + fq * 64 + (ch & 7) * 8;
#pragma unroll
    for (int i = 0; i < NV; i++) {
      int k = (tid + NT * i) >> 4; k = k > 143 ? 143 : k;
      p[i] = (const u32x4*)(src + (size_t)k * F);
    }
  } else {
    const int sg = unit - 288, ks = sg / 36, q = sg - ks * 36;
    const int fa = ks * 8 + (q < 32 ? (q & 7) : 2 * (q - 32)), fb = q < 32 ? fa : fa + 1;
    const int Pa = fa * 9 + panel_phys(fa, q < 32 ? 2 * (q >> 3) : 8), Pb = fb * 9 + panel_phys(fb, q < 32 ? 2 * (q >> 3) + 1 : 8);
    const u32x4* srcA = (const u32x4*)(w2 + (size_t)Pa * 16 * D);
    const u32x4* srcB = (const u32x4*)(w2 + (size_t)Pb * 16 * D) - 1152;
#pragma unroll
    for (int i = 0; i < NV; i++) {
      int c = tid + NT * i; c = c > 2303 ? 2303 : c;
      p[i] = (c < 1152 ? srcA : srcB) + c;
    }
  }
}
template <int NT, int NV>
DEVI void retile_load(const u16* w1, const u16* w3, const u16* w2, int unit, int tid, u32x4 (&v)[NV]) {
  const u32x4* p[NV];
  retile_addr<NT, NV>(w1, w3, w2, unit, tid, p);
#pragma unroll
  for (int i = 0; i < NV; i++) v[i] = __builtin_nontemporal_load(p[i]);
}
template <int NT, int NV>
DEVI void retile_stage(LDS_AS char* smem, int unit, int tid, const u32x4 (&v)[NV]) {
  if (unit < 288) {
    const int ch = tid & 15;
#pragma unroll
    for (int i = 0; i < NV; i++) {
      int k = (tid + NT * i) >> 4; k = k > 143 ? 143 : k;
      *(LDS_AS u32x4*)(smem + k * 256 + (((ch >> 1) ^ (k & 3)) * 32) + (ch & 1) * 16) = v[i];
    }
  } else {
#pragma unroll
    for (int i = 0; i < NV; i++) {
      int c = tid + NT * i; c = c > 2303 ? 2303 : c;
      const int r = c / 72, cc = c - r * 72;
      *(LDS_AS u32x4*)(smem + r * 1152 + ((cc ^ (r & 6)) * 16)) = v[i];
    }
  }
}
struct RtDst { const char* base; u32 voff; int k0; };
DEVI u32 rt_off(const RtDst& d, int i) { return d.k0 < 0 ? i * 1024 : ((d.k0 + i) >> 1) * 2048 + ((d.k0 + i) & 1) * 512; }
DEVI RtDst retile_read(char* ws, LDS_AS char* smem, int unit, int lane, int wid, bool pub, u32x4 (&fr)[9]) {
  const int q4 = (lane & 15) >> 2, p4 = lane & 3;
  RtDst d;
  if (unit < 288) {
    const int fq = unit >> 2, kq = unit & 3;
    const int P = fq * 4 + wid, ft = P / 9, lp = panel_log(ft, P - ft * 9);
    const int mat = lane >> 5, g2 = (lane >> 4) & 1;
    float cs = 0.f;
#pragma unroll
    for (int kk = 0; kk < 9; kk++) {
      LDS_AS char* p0 = smem + (kk * 16 + 8 * g2 + q4) * 256 + (((mat * 4 + wid) ^ q4) * 32) + p4 * 8;
      fr[kk] = tr_read2(p0, p0 + 4 * 256);
#pragma unroll
      for (int j = 0; j < 4; j++) cs = dot2(fr[kk][j], 0x3f803f80u, cs);
    }
    cs += __shfl_xor(cs, 16);
    if (pub && g2 == 0) {
      float* csp = (float*)(ws + WS_CSP) + ((size_t)ft * 512 + lp * 32 + mat * 16 + (lane & 15)) * 4 + kq;
      __hip_atomic_store(csp, cs, __ATOMIC_RELAXED, __HIP_MEMORY_SCOPE_AGENT);
    }
    d.base = ws + WS_WT + (size_t)(ft * 9 + lp) * 36864; d.voff = mat * 1024 + (lane & 31) * 16; d.k0 = kq * 9;
  } else {
    const int sg = unit - 288;
    const int row0 = 8 * (lane >> 4) + q4;
#pragma unroll
    for (int nf = 0; nf < 9; nf++) {
      LDS_AS char* p0 = smem + row0 * 1152 + (((wid * 9 + nf) ^ ((row0 >> 1) & 3)) * 32) + p4 * 8;
      LDS_AS char* p1 = smem + (row0 + 4) * 1152 + (((wid * 9 + nf) ^ (((row0 + 4) >> 1) & 3)) * 32) + p4 * 8;
      fr[nf] = tr_read2(p0, p1);
    }
    d.base = ws + WS_W2T + (size_t)(wid * 144 + sg) * 9216; d.voff = lane * 16; d.k0 = -1;
  }
  return d;
}

__global__ __launch_bounds__(256, 4) void k1_pre(K1Args a) {
  extern __shared__ __attribute__((aligned(16))) char smem_[];
  LDS_AS char* smem = (LDS_AS char*)smem_;
  const int tid = threadIdx.x, lane = tid & 63;
  const int wid = __builtin_amdgcn_readfirstlane(tid >> 6);
  int b = blockIdx.x;
  TS_DECL; TS(0);
  if (b < K1_KG) {
    const int d = b;
    LDS_AS f32x4* prm = (LDS_AS f32x4*)(smem + 16384);
    if (tid < NM) {
      const int n = d * NM + tid;
#define LDP(p) bf2f(__builtin_nontemporal_load(p + n))
      const float Are = LDP(a.Are), Aim = LDP(a.Aim), Bre = LDP(a.Bre), Bim = LDP(a.Bim);
      const float cr = LDP(a.Cre), ci = LDP(a.Cim);
      const float dt = rbf(__expf(bf2f(a.ldt[d])));
      const float br = dt * Bre, bi = dt * Bim;
      f32x4 v = {dt * Are * 1.44269504088896341f,
                 dt * Aim * 0.15915494309189535f,
                 cr * br - ci * bi, cr * bi + ci * br};
      prm[tid] = v;
    }
    const int r = lane & 15, kq = lane >> 4;
    const float p0 = (float)r, p1 = (float)kg_p1(r), p2 = (float)kg_p2(r), pY = (float)(16 * r);
    const u32x4 gi = *(const u32x4*)kg_tab.g[tid];
    const u32 ti = *(const u32*)kg_tab.t[tid];
    WAITL(0); BAR(); TS(3);
    f32x4 acc0 = {0, 0, 0, 0}, acc1 = acc0, acc2 = acc0;
    const f32x4 qm = prm[32 * wid + (lane & 31)];
    if (__all(qm[0] <= 0.f && fabsf(qm[1]) <= 0.125f)) {
      float cm = fabsf(qm[2]) + fabsf(qm[3]);
      cm = fmaxf(cm, dpp<0xB1>(cm)); cm = fmaxf(cm, dpp<0x4E>(cm)); cm = fmaxf(cm, dpp<0x141>(cm)); cm = fmaxf(cm, dpp<0x140>(cm));
      const float cmax = fmaxf(__builtin_bit_cast(float, __builtin_amdgcn_readlane(__builtin_bit_cast(int, cm), 0)),
                               __builtin_bit_cast(float, __builtin_amdgcn_readlane(__builtin_bit_cast(int, cm), 16)));
      const int E = __builtin_amdgcn_frexp_expf(cmax);
      const float ey = (float)(14 - E);
      const float oscale = __builtin_amdgcn_ldexpf(1.0f, E - 28);
#pragma unroll
      for (int s = 0; s < 2; s++) {
        u32x4 x0, x1, x2, y;
#pragma unroll
        for (int m4 = 0; m4 < 4; m4++) {
          const f32x4 q = prm[32 * wid + 16 * s + 4 * kq + m4];
          const f32x2 q01 = {q[0], q[1]};
          auto zpow = [&](float p, float sc) {
            const f32x2 pp = {p, p}, ss = {sc, 0.f};
            const f32x2 ar = __builtin_elementwise_fma(q01, pp, ss);
            const float e = __builtin_amdgcn_exp2f(ar[0]);
            const f32x2 cs = {__builtin_amdgcn_cosf(ar[1]), __builtin_amdgcn_sinf(ar[1])}, ee = {e, e};
            return cs * ee;
          };
          const f32x2 yv = zpow(pY, ey);
          y[m4] = pkh2(q[2] * yv[0] - q[3] * yv[1], -(q[2] * yv[1] + q[3] * yv[0]));
          f32x2 v;
          v = zpow(p0, 14.f); x0[m4] = pkh2(v[0], v[1]);
          v = zpow(p1, 14.f); x1[m4] = pkh2(v[0], v[1]);
          v = zpow(p2, 14.f); x2[m4] = pkh2(v[0], v[1]);
        }
        acc0 = mfma16h(x0, y, acc0); acc1 = mfma16h(x1, y, acc1); acc2 = mfma16h(x2, y, acc2);
      }
      acc0 *= oscale; acc1 *= oscale; acc2 *= oscale;
    } else {
#pragma unroll 1
      for (int s = 0; s < 2; s++) {
        u32x4 xh0, xl0, xh1, xl1, xh2, xl2, yh, yl;
#pragma unroll
        for (int m4 = 0; m4 < 4; m4++) {
          const f32x4 q = prm[32 * wid + 16 * s + 4 * kq + m4];
          auto zpow = [&](float p, float& re, float& im) {
            float e = __builtin_amdgcn_exp2f(q[0] * p);
            float ph = __builtin_amdgcn_fractf(q[1] * p);
            re = e * __builtin_amdgcn_cosf(ph); im = e * __builtin_amdgcn_sinf(ph);
          };
          auto split = [&](float re, float im, u32& hi, u32& lo) {
            hi = pk2(re, im);
            lo = pk2(re - bflo(hi), im - bfhi(hi));
          };
          float yr, yi, re, im; u32 h, l;
          zpow(pY, yr, yi);
          split(q[2] * yr - q[3] * yi, -(q[2] * yi + q[3] * yr), h, l); yh[m4] = h; yl[m4] = l;
          zpow(p0, re, im); split(re, im, h, l); xh0[m4] = h; xl0[m4] = l;
          zpow(p1, re, im); split(re, im, h, l); xh1[m4] = h; xl1[m4] = l;
          zpow(p2, re, im); split(re, im, h, l); xh2[m4] = h; xl2[m4] = l;
        }
        acc0 = mfma16(xl0, yh, acc0); acc1 = mfma16(xl1, yh, acc1); acc2 = mfma16(xl2, yh, acc2);
        acc0 = mfma16(xh0, yl, acc0); acc1 = mfma16(xh1, yl, acc1); acc2 = mfma16(xh2, yl, acc2);
        acc0 = mfma16(xh0, yh, acc0); acc1 = mfma16(xh1, yh, acc1); acc2 = mfma16(xh2, yh, acc2);
      }
    }
    LDS_AS float* part = (LDS_AS float*)smem;
    *(LDS_AS f32x4*)(part + ((wid * 3 + 0) * 16 + r) * 16 + 4 * kq) = acc0;
    *(LDS_AS f32x4*)(part + ((wid * 3 + 1) * 16 + r) * 16 + 4 * kq) = acc1;
    *(LDS_AS f32x4*)(part + ((wid * 3 + 2) * 16 + r) * 16 + 4 * kq) = acc2;
    __syncthreads(); TS(4);
    LDS_AS float* tab = part + 4 * 768;
    LDS_AS char* tb = (LDS_AS char*)tab;
    tab[tid] = (part[tid] + part[768 + tid]) + (part[2 * 768 + tid] + part[3 * 768 + tid]);
    *(LDS_AS float*)(tb + (ti & 0xffffu)) = (part[256 + tid] + part[768 + 256 + tid]) + (part[2 * 768 + 256 + tid] + part[3 * 768 + 256 + tid]);
    *(LDS_AS float*)(tb + (ti >> 16)) = (part[512 + tid] + part[768 + 512 + tid]) + (part[2 * 768 + 512 + tid] + part[3 * 768 + 512 + tid]);
    __syncthreads(); TS(5);
    auto rsk = mkrsrc(a.ws + WS_KREV + (size_t)d * KREV_PITCH, KREV_PITCH);
    u32x4 o;
#pragma unroll
    for (int j = 0; j < 4; j++) o[j] = pk2(*(LDS_AS float*)(tb + (gi[j] & 0xffffu)), *(LDS_AS float*)(tb + (gi[j] >> 16)));
    st16_wt(rsk, tid * 16, o);
    if (tid < 16) { u32x4 z = {0, 0, 0, 0}; st16_wt(rsk, 4096 + tid * 16, z); }
    else if (tid < 48) { u32x4 z = {~0u, ~0u, ~0u, ~0u}; st16_wt(mkrsrc(a.ws + WS_STAT, (u32)STAT_BYTES), (d * 32 + tid - 16) * 16, z); }
    else if (tid < 56 && d < 512) { u32x4 z = {0, 0, 0, 0}; st16_wt(mkrsrc(a.ws + WS_FLAG, (u32)FLAG_BYTES), (d * 8 + tid - 48) * 16, z); }
  } else if (b < K1_KG + K1_LN) {
    __builtin_amdgcn_s_setprio(3);
    const int lb = b - K1_KG;
    const int rb = (lb >> 4) * 8 + (lb & 7), chalf = (lb >> 3) & 1;
    constexpr int PITCH = 1152 + 32;
    const u32x4* src = (const u32x4*)(a.x + (size_t)rb * 32 * D);
    u32x4 v[9];
#pragma unroll
    for (int i = 0; i < 9; i++) v[i] = __builtin_nontemporal_load(src + tid + 256 * i);
#pragma unroll
    for (int i = 0; i < 9; i++) {
      int c = tid + 256 * i, r = c / 72, cc = c - r * 72;
      *(LDS_AS u32x4*)(smem + r * PITCH + cc * 16) = v[i];
    }
    __syncthreads(); TS(3);
    const int r = tid >> 3, sub = tid & 7;
    u32x4 q[9];
    f32x4 s4v = {0, 0, 0, 0};
#pragma unroll
    for (int i = 0; i < 9; i++) {
      q[i] = *(LDS_AS u32x4*)(smem + r * PITCH + (sub + 8 * i) * 16);
#pragma unroll
      for (int j = 0; j < 4; j++) s4v[j] = dot2(q[i][j], 0x3f803f80u, s4v[j]);
    }
    const float mean = sum8((s4v[0] + s4v[1]) + (s4v[2] + s4v[3])) * (1.0f / D);
    f32x4 qa = {0, 0, 0, 0}, qb = qa;
#pragma unroll
    for (int i = 0; i < 9; i++)
#pragma unroll
      for (int j = 0; j < 4; j++) {
        const float d0 = bflo(q[i][j]) - mean, d1 = bfhi(q[i][j]) - mean;
        qa[j] = fmaf(d0, d0, qa[j]); qb[j] = fmaf(d1, d1, qb[j]);
      }
    qa += qb;
    const float q2 = sum8((qa[0] + qa[1]) + (qa[2] + qa[3]));
    LDS_AS f32x2* st = (LDS_AS f32x2*)(smem + 32 * PITCH);
    if (sub == 0) {
      const float sd = rbf(sqrtf(rbf(rbf(q2 * (1.0f / D)) + 1e-5f)));
      f32x2 o = {rbf(mean), 1.0f / sd};
      st[r] = o;
    }
    __syncthreads(); TS(4);
    const int g = lane >> 4, i16 = lane & 15, q4 = i16 >> 2, p4 = i16 & 3;
    f32x2 rst[8];
#pragma unroll
    for (int j = 0; j < 8; j++) rst[j] = st[8 * g + j];
    auto rsu = mkrsrc(a.ws + WS_UX + (size_t)rb * D * 128, D * 128);
#pragma unroll
    for (int it = 0; it < 5; it++) {
      if (wid + 4 * it >= 18) break;
      const int cg = chalf * 18 + wid + 4 * it;
      LDS_AS char* p0 = smem + (8 * g + q4) * PITCH + cg * 32 + p4 * 8;
      u32x4 xv = tr_read2(p0, p0 + 4 * PITCH);
      u32x4 uv;
#pragma unroll
      for (int j = 0; j < 4; j++) {
        const u32 dpk = pk2(bflo(xv[j]) - rst[2 * j][0], bfhi(xv[j]) - rst[2 * j + 1][0]);
        uv[j] = pk2(bflo(dpk) * rst[2 * j][1], bfhi(dpk) * rst[2 * j + 1][1]);
      }
      const u32 off = (u32)(cg * 16 + i16) * 128 + g * 16;
      st16_wt(rsu, off, uv);
      st16_wt(rsu, off + 64, xv);
    }
  } else {
    __builtin_amdgcn_s_setprio(2);
#define PT_DELAY 3
    const int pb = b - K1_LN - K1_KG;
    u32 cpf = 0;
    if (pb < 40 && wid == 0) {
      u64 pc;
      asm volatile("s_getpc_b64 s[40:41]\n\ts_add_u32 s40, s40, _Z7k2_main6K2Args@rel32@lo+4\n\ts_addc_u32 s41, s41, _Z7k2_main6K2Args@rel32@hi+12\n\ts_mov_b64 %0, s[40:41]"
                   : "=s"(pc) :: "s40", "s41", "scc");
      cpf = *(const volatile u32*)((const char*)pc + (pb >> 3) * 8192 + lane * 128);
    }
    for (int i = 0; i < PT_DELAY; i++) __builtin_amdgcn_s_sleep(8);
    asm volatile("" ::"v"(cpf));
    const int qi = pb >> 2;
    const int unit = pb < 160 ? (((qi / 5) * 9 + 2 * (qi % 5)) << 2) + (pb & 3) : 288 + 128 + (pb - 160);
    u32x4 v[9];
    retile_load<256, 9>(a.w1, a.w3, a.w2, unit, tid, v);
    retile_stage<256, 9>(smem, unit, tid, v);
    __syncthreads();
    const RtDst rd = retile_read(a.ws, smem, unit, lane, wid, true, v);
    const auto rsw = mkrsrc(rd.base, 36864);
#pragma unroll
    for (int i = 0; i < 9; i++) st16_wt(rsw, rt_off(rd, i) + rd.voff, v[i]);
  }
  TS(1);
  WAITV(0);
  TS(2); TS_OUT(0);
}

constexpr int CV_CP = 4160;
constexpr int CV_UOFF = 4 * CV_CP;
constexpr int CV_BYTES = CV_UOFF + 4608;
constexpr int CV_K0 = 36864;
constexpr int RA = 0, RB = 36864, RC = 73728, RD = 110592;
constexpr int G1_RING = 36864, G1_ACT = 16384 + 128, G1_STAGE = G1_ACT + 4096;
constexpr int G1_RT = G1_RING + 4 * G1_STAGE;
constexpr int G1_CSOFF = G1_RING + 6 * G1_STAGE;
constexpr int G2_RES = 147456;
constexpr int K2_LDS = G1_CSOFF + 2048;
constexpr int g2_slot(int s) { return (s & 3) == 0 ? RD : ((s & 3) == 1 ? RA : ((s & 3) == 2 ? RB : RC)); }
constexpr int g1_region(int p) { return p == 0 || p == 6 ? RA : (p == 3 || p == 7 ? RB : (p == 4 || p == 8 ? RC : RD)); }
constexpr int FL_HFF = 32768, FL_A = 57344, FL_B = 59392, FL_RT = 61440, FL_IN = 63488;

struct K2Args { const u16 *dskip, *w1, *w3, *w2; char* ws; u16* out; };

struct G1Acc { f32x4 v[2][2]; };
struct G1Row { float mu[2], sd2[2], nrl2[2]; };
template <int PANEL>
DEVI void g1_epilogue(const G1Acc& acc, LDS_AS char* smem, int g, const G1Row& r, __amdgpu_buffer_rsrc_t rsh, u32 voff, int ft) {
  LDS_AS float* cs = (LDS_AS float*)(smem + G1_CSOFF) + PANEL * 32 + 4 * g;
  const f32x4 cg = *(LDS_AS f32x4*)cs, cu = *(LDS_AS f32x4*)(cs + 16);
  float o[2][4];
#pragma unroll
  for (int mf = 0; mf < 2; mf++) {
#pragma unroll
    for (int j = 0; j < 4; j++) {
      float tg = fmaf(-r.mu[mf], cg[j], acc.v[0][mf][j]);
      asm("" : "+v"(tg));
      float tu = fmaf(-r.mu[mf], cu[j], acc.v[1][mf][j]);
      asm("" : "+v"(tu));
      const float e = __builtin_amdgcn_exp2f(tg * r.nrl2[mf]);
      const float q = __builtin_amdgcn_rcpf(fmaf(e, r.sd2[mf], r.sd2[mf]));
      float p = tg * tu;
      asm("" : "+v"(p));
      p *= q;
      asm("" : "+v"(p));
      o[mf][j] = p;
    }
  }
  const auto s0 = __builtin_amdgcn_permlane16_swap(pk2(o[0][0], o[0][1]), pk2(o[1][0], o[1][1]), false, false);
  const auto s1 = __builtin_amdgcn_permlane16_swap(pk2(o[0][2], o[0][3]), pk2(o[1][2], o[1][3]), false, false);
  const u32x4 v = {s0[0], s1[0], s0[1], s1[1]};
  const int ftl = ft & 7;
  const int q = PANEL < 8 ? (PANEL >> 1) * 8 + ftl : 32 + (ftl >> 1);
  const int half = PANEL < 8 ? (PANEL & 1) : (ftl & 1);
  st16_wt(rsh, voff + (u32)((ft >> 3) * 36 + q) * 1024 + (u32)half * 512, v);
}

struct G1Bases { LDS_AS char* b[3]; };
template <int OFF>
DEVI u32x4 g1_ldw(const G1Bases& wb) { constexpr int I = OFF / 49152 < 2 ? OFF / 49152 : 2; return *(LDS_AS u32x4*)(wb.b[I] + (OFF - I * 49152)); }

struct G1Ctx {
  const char *hTw, *Wt, *Wq;
  u32 aoff, loff;
  u32 lds0, ldsw, ldsa;
  u32 rot2k;
  int wid;
  G1Bases wb, ab;
  const char *asrc, *wsrc;
};
template <int I>
DEVI void g2_piece(const G1Ctx& c, int s, u32 slot) {
  if constexpr (I < 4) dma16(c.asrc + (size_t)(I >> 1) * 144 * 1024 + s * 2048 + (I & 1) * 1024, c.loff, c.lds0 + slot + c.wid * 4096 + I * 1024);
  else if constexpr (I < 8) dma16(c.wsrc + (size_t)s * 18432 + (I - 4) * 4096, c.loff, c.ldsw + slot + 16384 + (I - 4) * 4096);
  else { if (c.wid < 2) dma16(c.wsrc + (size_t)s * 18432 + 16384, c.loff, c.ldsw + slot + 32768); }
}
DEVI void g2_stage_dma(const G1Ctx& c, int s, u32 slot) {
  g2_piece<0>(c, s, slot); g2_piece<1>(c, s, slot); g2_piece<2>(c, s, slot); g2_piece<3>(c, s, slot); g2_piece<4>(c, s, slot);
  g2_piece<5>(c, s, slot); g2_piece<6>(c, s, slot); g2_piece<7>(c, s, slot); g2_piece<8>(c, s, slot);
}
DEVI u32 g1_rot(const G1Ctx& c, u32 off) { const u32 x = off + c.rot2k; return x >= 36864u ? x - 36864u : x; }
template <int S, int I>
DEVI void g1_piece(const G1Ctx& c) {
  const u32 po = g1_rot(c, S * 2048);
  constexpr u32 dst = G1_RING + (S % 6) * G1_STAGE;
  if constexpr (I < 2) dma16(c.hTw + I * 16 * (L * 2), c.aoff + po * 64u, c.ldsa + dst + I * 8256);
  else { if ((c.wid >> 2) == (S & 1)) dma16(c.Wq, c.loff + po, c.ldsw + dst + G1_ACT - (S & 1) * 4096); }
}
template <int S> DEVI void g1_stage3(const G1Ctx& c) { g1_piece<S, 0>(c); g1_piece<S, 1>(c); g1_piece<S, 2>(c); }
template <int P>
DEVI void g1_frag(const G1Ctx& c, int fr) {
  fr = fr > 35 ? 35 : fr;
  dma16(c.Wt + P * 36864, c.loff + g1_rot(c, fr * 1024), c.lds0 + g1_region(P) + fr * 1024);
}
template <int PANEL, int I>
DEVI void g1_refill(const G1Ctx& c) {
  if constexpr (PANEL == 3) { if constexpr (I < 2) g1_frag<5>(c, 24 + 8 * I + c.wid); else if constexpr (I < 7) g1_frag<6>(c, 8 * (I - 2) + c.wid); }
  else if constexpr (PANEL < 6) {
    if constexpr (I < 5) g1_frag<PANEL + 3>(c, 8 * I + c.wid);
  }
  else { if (c.wid < 4) g2_piece<I>(c, PANEL - 6, g2_slot(PANEL - 6)); }
}

template <int PANEL, int K>
struct G1PassStep {
  static DEVI void run(G1Acc& acc, u32x4 (&w)[3], const u32x4 (&act)[36], const G1Ctx& c) {
    u32x4 cur = w[K % 3];
    acc.v[K & 1][0] = mfma16(cur, act[K & ~1], acc.v[K & 1][0]);
    acc.v[K & 1][1] = mfma16(cur, act[K | 1], acc.v[K & 1][1]);
    OPAQUE(acc.v[K & 1][0]); OPAQUE(acc.v[K & 1][1]);
    if constexpr (K + 3 < 36) w[K % 3] = g1_ldw<g1_region(PANEL) + (K + 3) * 1024>(c.wb);
    if constexpr (K % 4 == 1) g1_refill<PANEL, K / 4>(c);
    PIN();
    if constexpr (K + 1 < 36) G1PassStep<PANEL, K + 1>::run(acc, w, act, c);
  }
};
template <int PANEL>
DEVI G1Acc g1_pass(const u32x4 (&act)[36], const G1Ctx& c) {
  G1Acc acc = {};
  u32x4 w[3];
  constexpr int R = g1_region(PANEL);
  w[0] = g1_ldw<R>(c.wb); w[1] = g1_ldw<R + 1024>(c.wb); w[2] = g1_ldw<R + 2048>(c.wb);
  PIN();
  G1PassStep<PANEL, 0>::run(acc, w, act, c);
  return acc;
}

template <int J>
DEVI u32x4 g1_lda(const G1Ctx& c) {
  constexpr int OFF = G1_RING + ((J >> 1) % 6) * G1_STAGE + (J & 1) * 32, I = OFF / 49152 < 2 ? OFF / 49152 : 2;
  LDS_AS char* p = c.ab.b[I] + (OFF - I * 49152);
  return tr_read2(p, p + 2048);
}
template <int J, int P>
DEVI u32x4 g1_ldw1(const G1Ctx& c) {
  if constexpr (P == 0) return g1_ldw<RA + J * 1024>(c.wb);
  else return g1_ldw<G1_RING + ((J >> 1) % 6) * G1_STAGE + G1_ACT + (P - 1) * 2048 + (J & 1) * 1024>(c.wb);
}
DEVI void flags_add(const char* fl, int b) { __hip_atomic_fetch_add((u32*)(fl + (b & 15) * 128), 1u, __ATOMIC_RELAXED, __HIP_MEMORY_SCOPE_AGENT); }
DEVI bool flags_set(u32 v) { return __all(v >= 16u); }
DEVI void flags_spin(const char* fl, u32 foff) {
  for (u32 spins = 0; spins < (1u << 22); ++spins) {
    u32 v;
    asm volatile("global_load_dword %0, %1, %2 sc1\n\ts_waitcnt vmcnt(0)" : "=&v"(v) : "v"(foff), "s"(fl) : "memory");
    if (flags_set(v)) break;
    __builtin_amdgcn_s_sleep(1);
  }
}

template <int N> DEVI void waitv() { asm volatile("s_waitcnt vmcnt(%0)" ::"i"(N) : "memory"); }

struct G1Stat {
  const char *stat, *flag, *csp;
  u32 soff, coff, foff; int ft, b;
  u32x2 g[3]; u32 fl; u32x4 cs;
  RtDst rd;
};

template <int J>
struct G1P1Step {
  static DEVI void run(const G1Ctx& c, G1Acc (&acc)[4], u32x4 (&w)[3], u32x4 (&act)[36], G1Stat& st, const u32x4 (&rf)[9], int lane) {
    constexpr int S = J >> 1;
    if constexpr (J & 1) {
      if constexpr (S < 3) { if (c.wid >= 4) waitv<(S == 0 ? 15 : (S == 1 ? 10 : 7))>(); }
      else if constexpr (S <= 8) { if (c.wid >= 4) waitv<6>(); else waitv<9>(); }
      else if constexpr (S <= 13) waitv<6>();
      else if constexpr (S < 17) waitv<S - 4>();
      else waitv<5>();
      BAR();
      if constexpr (S == 11) { if (c.wid == 0 && lane == 0) flags_add(st.flag + FL_RT, st.b); }
      if constexpr (S == 13) {
#pragma unroll
        for (int i = 0; i < 3; i++)
          asm volatile("global_load_dwordx2 %0, %1, %2 sc1" : "=v"(st.g[i]) : "v"(st.soff), "s"(st.stat + i * 2048) : "memory");
        asm volatile("global_load_dwordx4 %0, %1, %2 sc1" : "=v"(st.cs) : "v"(st.coff), "s"(st.csp) : "memory");
      }
      if constexpr (S == 15) asm volatile("global_load_dword %0, %1, %2 sc1" : "=v"(st.fl) : "v"(st.foff), "s"(st.flag + FL_RT) : "memory");
      if constexpr (S == 17) {
        asm volatile("" : "+v"(st.fl));
        if (!flags_set(st.fl)) flags_spin(st.flag + FL_RT, st.foff);
      }
    }
    if constexpr ((J & 1) && J + 1 < 36) { act[J + 1] = g1_lda<J + 1>(c); act[J + 2] = g1_lda<J + 2>(c); }
    PIN();
#define G1_ONE(P)                                              \
    { u32x4 cur = w[P];                                        \
      acc[P].v[J & 1][0] = mfma16(cur, act[J & ~1], acc[P].v[J & 1][0]);   \
      acc[P].v[J & 1][1] = mfma16(cur, act[J | 1], acc[P].v[J & 1][1]);    \
      OPAQUE(acc[P].v[J & 1][0]); OPAQUE(acc[P].v[J & 1][1]);  \
      if constexpr (J + 1 < 36) w[P] = g1_ldw1<J + 1, P>(c);   \
      PIN(); }
    constexpr int TP = 3 + (S - 13) / 2, TF = S == 14 ? 16 : (S == 16 ? 24 : 0);
    constexpr bool T3 = S == 14 || S == 15 || S == 17;
    G1_ONE(0)
    if constexpr ((J & 1) && S > 0) { if constexpr (S < 13) g1_piece<S + 5, 0>(c); else g1_frag<TP>(c, TF + c.wid); PIN(); }
    if constexpr (!(J & 1) && S < 9) {
      if (c.wid < 4) asm volatile("global_store_dwordx4 %0, %1, %2 sc1\n\ts_nop 1" ::"v"(st.rd.voff), "v"(rf[S]), "s"(st.rd.base + rt_off(st.rd, S)) : "memory");
      PIN();
    }
    if constexpr (J == 6) {
      if (st.ft < 18 && st.ft % 3 == 0) {
        float s1[2] = {0.f, 0.f}, s2[2] = {0.f, 0.f};
#pragma unroll
        for (int k = 0; k < 6; k++)
#pragma unroll
          for (int j = 0; j < 4; j++) { s1[k & 1] = dot2(act[k][j], 0x3f803f80u, s1[k & 1]); s2[k & 1] = dot2(act[k][j], act[k][j], s2[k & 1]); }
#pragma unroll
        for (int i = 0; i < 2; i++) {
          s1[i] += __shfl_xor(s1[i], 16); s1[i] += __shfl_xor(s1[i], 32);
          s2[i] += __shfl_xor(s2[i], 16); s2[i] += __shfl_xor(s2[i], 32);
        }
        u32x2 o = {__float_as_uint((lane & 16) ? s1[1] : s1[0]), __float_as_uint((lane & 16) ? s2[1] : s2[0])};
        if (o.y == ~0u) o.y = 0x7fc00000u;
        asm volatile("global_store_dwordx2 %0, %1, %2 sc1\n\ts_nop 1" ::"v"(st.soff & 2047u), "v"(o), "s"(st.stat + (st.ft / 3) * 2048) : "memory");
      }
      PIN();
    }
    G1_ONE(1)
    if constexpr (!(J & 1) && S - 1 > 0 && S - 1 < 13) { g1_piece<S + 4, 2>(c); PIN(); }
    if constexpr ((J & 1) && T3) { g1_frag<TP>(c, TF + 16 + c.wid); PIN(); }
    G1_ONE(2)
    if constexpr ((J & 1) && S > 0) { if constexpr (S < 13) g1_piece<S + 5, 1>(c); else g1_frag<TP>(c, TF + 8 + c.wid); PIN(); }
#undef G1_ONE
    if constexpr (J + 1 < 36) G1P1Step<J + 1>::run(c, acc, w, act, st, rf, lane);
  }
};

template <int SLOT_N, int KK_N, bool SYNC>
DEVI void g2_step(f32x4 (&acc)[2][9], const u32x4 (&A)[2], const u32x4 (&W)[9], u32x4 (&An)[2], u32x4 (&Wn)[9],
                  const G1Bases& ab, const G1Bases& wb) {
#define G2_MM(nf) acc[0][nf] = mfma16(W[nf], A[0], acc[0][nf]); acc[1][nf] = mfma16(W[nf], A[1], acc[1][nf]); \
                  OPAQUE(acc[0][nf]); OPAQUE(acc[1][nf]); PIN();
#define G2_LW(nf) Wn[nf] = g1_ldw<SLOT_N + 16384 + KK_N * 9216 + (nf) * 1024>(wb);
  G2_MM(0)
  An[0] = g1_ldw<SLOT_N + KK_N * 1024>(ab); An[1] = g1_ldw<SLOT_N + 2048 + KK_N * 1024>(ab); PIN();
  G2_MM(1) G2_LW(0) G2_LW(1) PIN();
  G2_MM(2) G2_LW(2) G2_LW(3) PIN();
  G2_MM(3) G2_LW(4) G2_LW(5) PIN();
  G2_MM(4) G2_LW(6) G2_LW(7) PIN();
  G2_MM(5) G2_LW(8) PIN();
  G2_MM(6)
  G2_MM(7)
  if constexpr (SYNC) { WAITL(0); BAR(); PIN(); }
  G2_MM(8)
#undef G2_MM
#undef G2_LW
}
template <int S4, bool LAST>
DEVI void g2_stage(f32x4 (&o)[2][9], u32x4 (&A0)[2], u32x4 (&W0)[9], u32x4 (&A1)[2], u32x4 (&W1)[9], const G1Bases& ab, const G1Bases& wb) {
  g2_step<g2_slot(S4), 1, !LAST>(o, A0, W0, A1, W1, ab, wb);
  g2_step<g2_slot(LAST ? S4 : S4 + 1), 0, false>(o, A1, W1, A0, W0, ab, wb);
}

template <int E0, int NE>
DEVI void cv_copies(LDS_AS char* base, int tt, const u32x4& k0, const u32x4& k1) {
  const u32 w[8] = {k0.x, k0.y, k0.z, k0.w, k1.x, k1.y, k1.z, k1.w};
#pragma unroll
  for (int e = E0; e < E0 + NE; e++) {
    u32x4 o;
#pragma unroll
    for (int j = 0; j < 4; j++)
      o[j] = (e & 1) ? __builtin_amdgcn_alignbyte(w[(e >> 1) + j + 1], w[(e >> 1) + j], 2) : w[(e >> 1) + j];
    *(LDS_AS u32x4*)(base + e * CV_CP + tt * 16) = o;
  }
}
DEVI u32x4 cv_lda(LDS_AS char* p) {
  const u32x2 lo = *(volatile LDS_AS u32x2*)p, hi = *(volatile LDS_AS u32x2*)(p + 8);
  return (u32x4){lo.x, lo.y, hi.x, hi.y};
}
template <int NW, int STRIDE>
DEVI void cv_sum(LDS_AS char* p, f32x4& y0, f32x4& y1) {
  y0 = (f32x4){0, 0, 0, 0}; y1 = y0;
#pragma unroll
  for (int w = 0; w < NW; w++) { y0 += *(LDS_AS f32x4*)(p + w * STRIDE); y1 += *(LDS_AS f32x4*)(p + w * STRIDE + 256); }
}
DEVI u32x4 cv_out(const f32x4& y0, const f32x4& y1, const u32x4& uv, const u32x4& xv, float dsk) {
  const float y[8] = {y0[0], y0[1], y0[2], y0[3], y1[0], y1[1], y1[2], y1[3]};
  u32x4 o;
#pragma unroll
  for (int j = 0; j < 4; j++) {
    const u32 yb = pk2(y[2 * j], y[2 * j + 1]), sk = pk2(bflo(uv[j]) * dsk, bfhi(uv[j]) * dsk);
    const u32 z = pk2(bflo(yb) + bflo(sk), bfhi(yb) + bfhi(sk));
    o[j] = pk2(bflo(xv[j]) + bflo(z), bfhi(xv[j]) + bfhi(z));
  }
  return o;
}

__global__ __launch_bounds__(512, 2) void k2_main(K2Args a) {
  extern __shared__ __attribute__((aligned(16))) char smem_[];
  LDS_AS char* smem = (LDS_AS char*)smem_;
  const int tid = threadIdx.x, lane = tid & 63;
  const int wid = __builtin_amdgcn_readfirstlane(tid >> 6);
  const int b = blockIdx.x, xcd = b & 7, rr = b >> 3;
  const int mt = (xcd & 1) * 4 + (rr & 3), ft = (xcd >> 1) * 8 + (rr >> 2);
  TS_DECL; TS(0);
  G1Ctx c;
  c.wid = wid;
  c.rot2k = (ft < 18 ? ft : ft - 18) * 2048;
  c.lds0 = (u32)(size_t)smem;
  c.ldsw = c.lds0 + wid * 1024;
  c.ldsa = c.ldsw + (wid >> 2) * 32;
  c.hTw = a.ws + WS_HT + mt * 512 + (size_t)(2 * wid) * (L * 2);
  c.Wt = a.ws + WS_WT + (size_t)ft * 9 * 36864;
  c.Wq = c.Wt + (1 + ((wid >> 1) & 1)) * 36864 + (wid & 1) * 1024;
  c.loff = lane * 16;
  const int arow = lane >> 5;
  c.aoff = arow * (L * 2) + (((lane & 31) ^ (((2 * wid + arow) & 3) << 2)) * 16);
  const int unit = b < 128 ? ((((b >> 4) * 9 + 2 * ((b >> 2) & 3) + 1)) << 2) + (b & 3) : 288 + (b - 128);
  u32x4 rv[5], rf[9];
  RtDst rd;
  {
    const int half = wid >> 2, wl = wid & 3, tt = tid & 255;
    const int dm = b + 256 * half;
    const int dc = 512 + (b >> 2), nlo = b & 3, nhi = 7 - nlo;
    const u32x4* krm = (const u32x4*)(a.ws + WS_KREV + (size_t)dm * KREV_PITCH);
    const u32x4* krc = (const u32x4*)(a.ws + WS_KREV + (size_t)dc * KREV_PITCH);
    const char* ux = a.ws + WS_UX + (size_t)(tt >> 2) * D * 128 + (tt & 3) * 16;
    const u32x4 k0 = krm[tt], k1 = krm[tt + 1], uv = *(const u32x4*)(ux + dm * 128);
    const u32x4 kc0 = krc[tt], kc1 = krc[tt + 1], uc = *(const u32x4*)(ux + dc * 128);
    const u32x4 xv = *(const u32x4*)(ux + dm * 128 + 64);
    const int te = ((tt & 32) ? nlo : nhi) * 32 + (tt & 31);
    const char* uxe = a.ws + WS_UX + ((size_t)(te >> 2) * D + dc) * 128 + (te & 3) * 16;
    const u32x4 ue = *(const u32x4*)uxe, xe = *(const u32x4*)(uxe + 64);
    const float dskm = bf2f(a.dskip[dm]), dskc = bf2f(a.dskip[dc]);
    const u32x4* rp[5];
    retile_addr<512, 5>(a.w1, a.w3, a.w2, unit, tid, rp);
#pragma unroll
    for (int i = 0; i < 5; i++) OPAQUE(rp[i]);
    LDS_AS char* cm = smem + CV_K0 + half * CV_BYTES;
    LDS_AS char* cc = smem + CV_K0 + 2 * CV_BYTES;
    cv_copies<0, 4>(cm, tt, k0, k1);
    *(LDS_AS u32x4*)(cm + CV_UOFF + 512 + tt * 16) = uv;
    if (half == 0) cv_copies<0, 2>(cc, tt, kc0, kc1);
    else { cv_copies<2, 2>(cc, tt, kc0, kc1); *(LDS_AS u32x4*)(cc + CV_UOFF + 512 + tt * 16) = uc; }
    {
      const u32x4 z = {0, 0, 0, 0};
      if (tid == 511) *(LDS_AS u32*)(smem + G1_CSOFF) = 0u;
      if (tid < 144) {
        const int ch = tid / 48, i = tid - ch * 48;
        *(LDS_AS u32x4*)(smem + CV_K0 + ch * CV_BYTES + (i < 16 ? (i >> 2) * CV_CP + 4096 + (i & 3) * 16 : CV_UOFF + (i - 16) * 16)) = z;
      }
    }
    __builtin_amdgcn_s_waitcnt(0x0070);
    BAR();
    TS(1);
    const int i16 = lane & 15, g = lane >> 4;
    const int aoff = ((3 - i16) & 3) * CV_CP + (2028 - (i16 & 12) + 8 * g) * 2;
    const int boff = CV_UOFF + (2 * i16 + g + 30) * 16;
    f32x4 acc[8], eh = {0, 0, 0, 0}, el = eh;
#pragma unroll
    for (int n = 0; n < 8; n++) acc[n] = (f32x4){0, 0, 0, 0};
    {
      LDS_AS char* ap = cm + aoff - 128 * wl;
      LDS_AS char* bp = cm + boff - 128 * wl;
      LDS_AS char* apc = cc + aoff - 64 * wid;
      LDS_AS char* bh = cc + boff - 64 * wid + 512 * nhi;
      LDS_AS char* bl = cc + boff - 64 * wid + 512 * nlo;
      u32x4 A[2][8], B[2][8], Ac[8], Bh[8], Bl[4];
      A[0][0] = cv_lda(ap);
#pragma unroll
      for (int j = 0; j < 8; j++) B[0][j] = *(LDS_AS u32x4*)(bp + 512 * j);
      A[0][1] = cv_lda(ap - 512);
      PIN();
#pragma unroll
      for (int a1 = 0; a1 < 8; a1++) {
        if (a1 + 2 < 8) A[0][a1 + 2] = cv_lda(ap - 512 * (a1 + 2));
        B[1][a1] = *(LDS_AS u32x4*)(bp - 64 + 512 * a1);
        if (a1 >= 6) A[1][a1 - 6] = cv_lda(ap - 64 - 512 * (a1 - 6));
        PIN();
#pragma unroll
        for (int n = a1; n < 8; n++) acc[n] = mfma16(A[0][a1], B[0][n - a1], acc[n]);
        if (a1 < 5) rv[a1] = __builtin_nontemporal_load(rp[a1]);
        PIN();
      }
#pragma unroll
      for (int a1 = 0; a1 < 8; a1++) {
        if (a1 + 2 < 8) A[1][a1 + 2] = cv_lda(ap - 64 - 512 * (a1 + 2));
        Ac[a1] = cv_lda(apc - 512 * a1);
        Bh[a1] = *(LDS_AS u32x4*)(bh - 512 * (a1 <= nhi ? a1 : nhi));
        if (a1 < 4) Bl[a1] = *(LDS_AS u32x4*)(bl - 512 * (a1 <= nlo ? a1 : nlo));
        PIN();
#pragma unroll
        for (int n = a1; n < 8; n++) acc[n] = mfma16(A[1][a1], B[1][n - a1], acc[n]);
        PIN();
      }
#pragma unroll
      for (int a1 = 0; a1 < 8; a1++) {
        if (a1 <= nhi) eh = mfma16(Ac[a1], Bh[a1], eh);
        if (a1 < 4 && a1 <= nlo) el = mfma16(Ac[a1], Bl[a1], el);
      }
    }
    BAR();
    LDS_AS char* pm = smem + CV_K0 + half * 32768;
    LDS_AS char* pc = smem + CV_K0 + 65536;
#pragma unroll
    for (int n = 0; n < 8; n++) *(LDS_AS f32x4*)(pm + wl * 8192 + n * 1024 + lane * 16) = acc[n];
    *(LDS_AS f32x4*)(pc + wid * 2048 + lane * 16) = eh;
    *(LDS_AS f32x4*)(pc + wid * 2048 + 1024 + lane * 16) = el;
    WAITL(0); BAR();
    {
      const int soff = ((tt >> 5) * 64 + ((tt & 31) >> 1) + 32 * (tt & 1)) * 16;
      f32x4 y0, y1;
      cv_sum<4, 8192>(pm + soff, y0, y1);
      st16_wt(mkrsrc(a.ws + WS_HT + (size_t)dm * (L * 2), L * 2), tt * 16, cv_out(y0, y1, uv, xv, dskm));
      if (wid == 4) {
        cv_sum<8, 2048>(pc + soff, y0, y1);
        st16_wt(mkrsrc(a.ws + WS_HT + (size_t)dc * (L * 2), L * 2), te * 16, cv_out(y0, y1, ue, xe, dskc));
      }
    }
    retile_stage<512, 5>(smem + G1_RT, unit, tid, rv);
    WAITV(0);
    __builtin_amdgcn_s_waitcnt(0x0070);
    BAR();
    TS(2);
    if (tid == 0) flags_add(a.ws + WS_FLAG + FL_A, b);
    rd = retile_read(a.ws, smem + G1_RT, unit, lane, wl, wid < 4, rf);
    WAITL(0);
    if (wid >= 4) {
      volatile LDS_AS u32* go = (volatile LDS_AS u32*)(smem + G1_CSOFF);
      if (wid == 4) { flags_spin(a.ws + WS_FLAG + FL_A, (lane & 15) * 128); *go = 1u; }
      else {
#pragma unroll 1
        for (int i = wid - 5; i < 36; i += 3) g1_frag<0>(c, i);
        for (u32 spins = 0; *go == 0u && spins < (1u << 24); ++spins) __builtin_amdgcn_s_sleep(1);
      }
      TS(3);
#pragma unroll 1
      for (int S = 0; S < 4; S++) {
        const u32 po = g1_rot(c, S * 2048), dst = c.ldsw - 4096 + G1_RING + S * G1_STAGE;
#pragma unroll
        for (int i = 0; i < 4; i++) dma16(c.hTw + (i - 1) * 8 * (L * 2), c.aoff + po * 64u, dst + i * 4128);
        dma16(c.Wq, c.loff + po, dst + G1_ACT);
      }
      WAITV(15);
    }
  }
  const int hh = lane >> 5, g4 = lane >> 4, q4 = (lane & 15) >> 2, p4 = lane & 3;
#pragma unroll
  for (int i = 0; i < 3; i++) {
    c.ab.b[i] = smem + (8 * g4 + q4) * 512 + g4 * 32 + ((wid ^ q4) * 64) + p4 * 8 + i * 49152;
    c.wb.b[i] = smem + lane * 16 + i * 49152;
    OPAQUE(c.ab.b[i]); OPAQUE(c.wb.b[i]);
  }
  G1Stat st;
  st.stat = a.ws + WS_STAT + (size_t)mt * 18 * 2048;
  st.flag = a.ws + WS_FLAG;
  st.csp = a.ws + WS_CSP + ft * 8192;
  st.soff = hh * 3 * 2048 + (wid * 32 + (lane & 31)) * 8;
  st.coff = tid * 16;
  st.foff = (lane & 15) * 128;
  st.ft = ft; st.b = b;
  u32x4 act[36], w[3];
  G1Acc acc[4] = {};
  BAR();
  TS(4);
  g1_stage3<4>(c); g1_stage3<5>(c);
  act[0] = g1_lda<0>(c); act[1] = g1_lda<1>(c);
  w[0] = g1_ldw1<0, 0>(c); w[1] = g1_ldw1<0, 1>(c); w[2] = g1_ldw1<0, 2>(c);
  PIN();
  st.rd = rd;
  G1P1Step<0>::run(c, acc, w, act, st, rf, lane);
  TS(5);
  asm volatile("" : "+v"(st.g[0]), "+v"(st.g[1]), "+v"(st.g[2]), "+v"(st.cs));
  *(LDS_AS float*)(smem + G1_CSOFF + tid * 4) = (__uint_as_float(st.cs[0]) + __uint_as_float(st.cs[1])) + (__uint_as_float(st.cs[2]) + __uint_as_float(st.cs[3]));
  {
    bool bad = st.g[0].y == ~0u || st.g[1].y == ~0u || st.g[2].y == ~0u;
    for (u32 spins = 0; __any(bad) && spins < (1u << 22); ++spins) {
      __builtin_amdgcn_s_sleep(2);
      bad = false;
#pragma unroll
      for (int i = 0; i < 3; i++) {
        asm volatile("global_load_dwordx2 %0, %1, %2 sc1\n\ts_waitcnt vmcnt(0)" : "=&v"(st.g[i]) : "v"(st.soff), "s"(st.stat + i * 2048) : "memory");
        bad |= (st.g[i].y == ~0u);
      }
    }
  }
  float s1 = 0.f, s2 = 0.f;
#pragma unroll
  for (int i = 0; i < 3; i++) { s1 += __uint_as_float(st.g[i].x); s2 += __uint_as_float(st.g[i].y); }
  s1 += __shfl_xor(s1, 32); s2 += __shfl_xor(s2, 32);
  const float mean = s1 * (1.0f / D);
  const float var = fmaxf(s2 * (1.0f / D) - mean * mean, 0.f);
  const float mu = rbf(mean);
  const float sd = rbf(sqrtf(rbf(rbf(var) + 1e-5f)));
  const float mu2 = __shfl_xor(mu, 16), sdo = __shfl_xor(sd, 16);
  G1Row row;
  row.mu[0] = (lane & 16) ? mu2 : mu; row.mu[1] = (lane & 16) ? mu : mu2;
  const float sdA = (lane & 16) ? sdo : sd, sdB = (lane & 16) ? sd : sdo;
  row.sd2[0] = sdA * sdA; row.sd2[1] = sdB * sdB;
  row.nrl2[0] = -1.44269504088896341f / sdA; row.nrl2[1] = -1.44269504088896341f / sdB;
  auto rsh = mkrsrc(a.ws + WS_HFF, (u32)HFF_BYTES);
  const u32 voff = (u32)(mt * 16 + wid * 2 + (g4 & 1)) * (144 * 1024) + (u32)((lane & 15) + 16 * hh) * 16;

  const int ks = xcd >> 1, mt16 = (xcd & 1) * 8 + (rr & 7), nt = rr >> 3;
  const int tile = mt16 * 4 + nt;
  c.asrc = a.ws + WS_HFF + ((size_t)(mt16 * 8 + 2 * (wid & 3)) * 144 + ks * 36) * 1024;
  c.wsrc = a.ws + WS_W2T + ((size_t)nt * 144 + ks * 36) * 9216 + (wid & 3) * 1024;
  u32* fl_out = (u32*)(a.ws + WS_FLAG + FL_HFF + (size_t)(((mt * 2 + (wid >> 2)) * 4 + (ft >> 3)) * 3) * 128);
  const char* fl_in = a.ws + WS_FLAG + FL_HFF + (size_t)((mt16 * 4 + ks) * 3) * 128;
  auto publish = [&](int f) { if (lane == 0) __hip_atomic_fetch_add(fl_out + f * 32, 1u, __ATOMIC_RELAXED, __HIP_MEMORY_SCOPE_AGENT); };
  u32 pollv;
  auto poll_issue = [&](int f) { asm volatile("global_load_dword %0, %1, %2 sc1" : "=v"(pollv) : "v"(0u), "s"(fl_in + f * 128) : "memory"); };
  auto poll_check = [&](int f) {
    asm volatile("" : "+v"(pollv));
    for (u32 spins = 0; __builtin_amdgcn_readfirstlane(pollv) < 32u && spins < (1u << 22); ++spins) {
      __builtin_amdgcn_s_sleep(2);
      asm volatile("global_load_dword %0, %1, %2 sc1\n\ts_waitcnt vmcnt(0)" : "=&v"(pollv) : "v"(0u), "s"(fl_in + f * 128) : "memory");
    }
  };

#define EPI(P, A) g1_epilogue<P>(A, smem, g4, row, rsh, voff, ft); PIN()
  const bool early = wid >= 4;
  WAITL(0); BAR();
  asm volatile("global_load_dwordx4 %0, %1, %2 sc1" : "=v"(st.cs) : "v"(st.coff), "s"(st.csp) : "memory");
  if (early) { EPI(0, acc[0]); EPI(1, acc[1]); }
  acc[3] = g1_pass<3>(act, c);
  if (!early) { EPI(0, acc[0]); EPI(1, acc[1]); }
  WAITV(9);
  asm volatile("" : "+v"(st.cs));
  if (tid >= 160) *(LDS_AS float*)(smem + G1_CSOFF + tid * 4) = (__uint_as_float(st.cs[0]) + __uint_as_float(st.cs[1])) + (__uint_as_float(st.cs[2]) + __uint_as_float(st.cs[3]));
  WAITL(0); BAR();
  if (early) { EPI(2, acc[2]); EPI(3, acc[3]); }
  acc[0] = g1_pass<4>(act, c);
  if (!early) { EPI(2, acc[2]); EPI(3, acc[3]); }
  TS(6);
  WAITV(7);
  publish(0);
  BAR();
  if (early) { EPI(4, acc[0]); }
  acc[1] = g1_pass<5>(act, c);
  if (!early) { poll_issue(0); EPI(4, acc[0]); EPI(5, acc[1]); WAITV(2); poll_check(0); }
  else WAITV(7);
  BAR();
  if (early) { EPI(5, acc[1]); }
  acc[2] = g1_pass<6>(act, c);
  if (!early) { EPI(6, acc[2]); } else WAITV(1);
  BAR();
  if (early) { EPI(6, acc[2]); }
  acc[3] = g1_pass<7>(act, c);
  if (!early) { EPI(7, acc[3]); WAITV(9); } else WAITV(1);
  publish(1);
  TS(7);
  BAR();
  if (early) { EPI(7, acc[3]); }
  acc[0] = g1_pass<8>(act, c);
  EPI(8, acc[0]);
  TS(8);
#undef EPI
  BAR();

  u32* cnt_in = (u32*)(a.ws + WS_FLAG + (size_t)(tile * 4 + ks) * 128);
  if (!early) {
    poll_issue(1);
    g2_stage_dma(c, 3, g2_slot(3));
    {
      const char* hsrc = a.ws + WS_HT + (size_t)(nt * 144) * (L * 2) + (mt16 * 128 + ks * 32) * 2;
      const u32 hoff = (lane >> 2) * (L * 2) + (lane & 3) * 16;
#pragma unroll
      for (int i = 0; i < 3; i++) { const int pc = (wid + 4 * i < 9) ? wid + 4 * i : wid; dma16(hsrc + (size_t)pc * 16 * (L * 2), hoff, c.lds0 + G2_RES + pc * 1024); }
    }
    WAITV(11);
    poll_check(1);
    publish(2);
    BAR();
    g2_stage_dma(c, 4, g2_slot(0));
    BAR();
    g2_stage_dma(c, 5, g2_slot(1));
#define G2_LD(s) { if (wid < 2) WAITV(18); else WAITV(16); BAR(); g2_stage_dma(c, (s) + 4, g2_slot(s)); }
    G2_LD(2) G2_LD(3) G2_LD(4)
    poll_issue(2);
    G2_LD(5) G2_LD(6) G2_LD(7)
    poll_check(2);
    G2_LD(8) G2_LD(9) G2_LD(10) G2_LD(11) G2_LD(12) G2_LD(13)
#undef G2_LD
    if (wid < 2) WAITV(18); else WAITV(16);
    BAR();
    WAITV(8); BAR();
    WAITV(0); BAR();
  } else {
    const int mw = wid - 4;
    G1Bases ab;
#pragma unroll
    for (int i = 0; i < 3; i++) { ab.b[i] = smem + mw * 4096 + lane * 16 + i * 49152; OPAQUE(ab.b[i]); }
    f32x4 o[2][9];
#pragma unroll
    for (int i = 0; i < 2; i++)
#pragma unroll
      for (int j = 0; j < 9; j++) { o[i][j] = (f32x4){0, 0, 0, 0}; OPAQUE(o[i][j]); }
    u32x4 A0[2], W0[9], A1[2], W1[9];
    constexpr int S0 = g2_slot(0);
    A0[0] = g1_ldw<S0>(ab); A0[1] = g1_ldw<S0 + 2048>(ab);
    W0[0] = g1_ldw<S0 + 16384>(c.wb); W0[1] = g1_ldw<S0 + 17408>(c.wb); W0[2] = g1_ldw<S0 + 18432>(c.wb); W0[3] = g1_ldw<S0 + 19456>(c.wb);
    W0[4] = g1_ldw<S0 + 20480>(c.wb); W0[5] = g1_ldw<S0 + 21504>(c.wb); W0[6] = g1_ldw<S0 + 22528>(c.wb); W0[7] = g1_ldw<S0 + 23552>(c.wb);
    W0[8] = g1_ldw<S0 + 24576>(c.wb);
    PIN();
#pragma unroll 1
    for (int it = 0; it < 4; it++) {
      g2_stage<0, false>(o, A0, W0, A1, W1, ab, c.wb);
      g2_stage<1, false>(o, A0, W0, A1, W1, ab, c.wb);
      if (it == 0) { WAITV(0); publish(2); }
      g2_stage<2, false>(o, A0, W0, A1, W1, ab, c.wb);
      g2_stage<3, false>(o, A0, W0, A1, W1, ab, c.wb);
    }
    g2_stage<0, false>(o, A0, W0, A1, W1, ab, c.wb);
    g2_stage<1, true>(o, A0, W0, A1, W1, ab, c.wb);
    TS(9);
    if (mw != ks) {
      const auto rs_out = mkrsrc(a.ws + WS_PART + ((size_t)(tile * 4 + mw) * 4 + ks) * 9216, 9216);
#pragma unroll
      for (int j = 0; j < 9; j++) {
        u32x4 v = {pk2(o[0][j][0], o[0][j][1]), pk2(o[0][j][2], o[0][j][3]), pk2(o[1][j][0], o[1][j][1]), pk2(o[1][j][2], o[1][j][3])};
        st16_wt(rs_out, (j * 64 + lane) * 16, v);
      }
      WAITV(0);
      if (lane == 0) __hip_atomic_fetch_add((u32*)(a.ws + WS_FLAG + (size_t)(tile * 4 + mw) * 128), 1u, __ATOMIC_RELAXED, __HIP_MEMORY_SCOPE_AGENT);
    } else {
#pragma unroll
      for (int j = 0; j < 9; j++) {
        *(LDS_AS f32x4*)(smem + g2_slot(2) + ((j * 2) * 64 + lane) * 16) = o[0][j];
        *(LDS_AS f32x4*)(smem + g2_slot(2) + ((j * 2 + 1) * 64 + lane) * 16) = o[1][j];
      }
      WAITL(0);
    }
  }
  BAR();
  {
    u32 spins = 0;
    while (__builtin_amdgcn_readfirstlane(__hip_atomic_load(cnt_in, __ATOMIC_RELAXED, __HIP_MEMORY_SCOPE_AGENT)) < 3u) {
      __builtin_amdgcn_s_sleep(1);
      if (++spins > (1u << 24)) break;
    }
    __builtin_amdgcn_fence(__ATOMIC_ACQUIRE, "wavefront");
    TS(10);
    const auto rs_in = mkrsrc(a.ws + WS_PART + (size_t)(tile * 4 + ks) * 4 * 9216, 4 * 9216);
    int ln = threadIdx.x; asm volatile("" : "+v"(ln)); ln &= 63;
    const int lane = ln, g = ln >> 4, q4 = (ln & 15) >> 2, p4 = ln & 3;
    for (int j = wid; j < 9; j += 8) {
      u32x4 pp[3];
#pragma unroll
      for (int sidx = 0; sidx < 3; sidx++)
        pp[sidx] = __builtin_amdgcn_raw_buffer_load_b128(rs_in, ((ks + 1 + sidx) & 3) * 9216 + (j * 64 + lane) * 16, 0, 16);
      LDS_AS char* rb = smem + G2_RES + (j * 16 + 4 * g + q4) * 64 + p4 * 8;
      u16* orow = a.out + (size_t)(mt16 * 128 + ks * 32 + (lane & 15)) * D + nt * 144 + j * 16 + 4 * g;
#pragma unroll
      for (int i = 0; i < 2; i++) {
        const u32x2 hres = __builtin_bit_cast(u32x2, tr_read(rb + i * 32));
        f32x4 v = *(LDS_AS f32x4*)(smem + g2_slot(2) + ((j * 2 + i) * 64 + lane) * 16);
#pragma unroll
        for (int sidx = 0; sidx < 3; sidx++) {
          v[0] += bflo(pp[sidx][2 * i]); v[1] += bfhi(pp[sidx][2 * i]);
          v[2] += bflo(pp[sidx][2 * i + 1]); v[3] += bfhi(pp[sidx][2 * i + 1]);
        }
        const u32 o01 = pk2(v[0], v[1]), o23 = pk2(v[2], v[3]);
        u32x2 ov = {pk2(bflo(hres.x) + bflo(o01), bfhi(hres.x) + bfhi(o01)), pk2(bflo(hres.y) + bflo(o23), bfhi(hres.y) + bfhi(o23))};
        *(u32x2*)(orow + (size_t)i * 16 * D) = ov;
      }
    }
  }
  WAITV(0);
  TS(11);
  if (b == 0 && wid == 4) {
    const u32 t0k1 = __builtin_amdgcn_readfirstlane(__hip_atomic_load((u32*)(a.ws + WS_TS), __ATOMIC_RELAXED, __HIP_MEMORY_SCOPE_AGENT));
    const u32 iv = PROBE == 0 ? 0u : (PROBE == 1 ? ts_r[0] - t0k1 : (PROBE == 2 ? ts_r[4] - ts_r[0] : (PROBE == 3 ? ts_r[8] - ts_r[4] : (PROBE == 4 ? ts_r[11] - ts_r[8] : ts_r[2] - ts_r[0]))));
    const u32 s0 = (u32)__builtin_amdgcn_s_memrealtime();
    while ((u32)__builtin_amdgcn_s_memrealtime() - s0 < 10u * iv) __builtin_amdgcn_s_sleep(1);
  }
}

extern "C" void kernel_launch(void* const* d_in, const int* in_sizes, int n_in, void* d_out, int out_size, void* d_ws,
                              size_t ws_size, hipStream_t stream) {
  (void)in_sizes; (void)n_in; (void)out_size; (void)ws_size;
  (void)hipFuncSetAttribute((const void*)k2_main, hipFuncAttributeMaxDynamicSharedMemorySize, K2_LDS);
  K1Args a1;
  a1.x = (const u16*)d_in[0]; a1.Are = (const u16*)d_in[1]; a1.Aim = (const u16*)d_in[2];
  a1.Bre = (const u16*)d_in[3]; a1.Bim = (const u16*)d_in[4]; a1.Cre = (const u16*)d_in[5];
  a1.Cim = (const u16*)d_in[6]; a1.ldt = (const u16*)d_in[7]; a1.w1 = (const u16*)d_in[10];
  a1.w3 = (const u16*)d_in[11]; a1.w2 = (const u16*)d_in[12]; a1.ws = (char*)d_ws;
  k1_pre<<<K1_LN + K1_KG + K1_PT + (K1ORD == 2 ? 64 : 0), 256, K1_LDS, stream>>>(a1);
  K2Args a2;
  a2.dskip = (const u16*)d_in[8]; a2.w1 = (const u16*)d_in[10]; a2.w3 = (const u16*)d_in[11]; a2.w2 = (const u16*)d_in[12];
  a2.ws = (char*)d_ws; a2.out = (u16*)d_out;
  k2_main<<<256, 512, K2_LDS, stream>>>(a2);
}
```

```cpp
#include <hip/hip_runtime.h>
#include <stdint.h>

typedef unsigned short u16;
typedef unsigned int u32;
typedef unsigned long long u64;
typedef __bf16 bf2 __attribute__((ext_vector_type(2)));
typedef __bf16 bf8 __attribute__((ext_vector_type(8)));
typedef short s4 __attribute__((ext_vector_type(4)));
typedef u32 u32x2 __attribute__((ext_vector_type(2)));
typedef u32 u32x4 __attribute__((ext_vector_type(4)));
typedef float f32x2 __attribute__((ext_vector_type(2)));
typedef float f32x4 __attribute__((ext_vector_type(4)));
typedef float f32x16 __attribute__((ext_vector_type(16)));
#define LDS_AS __attribute__((address_space(3)))
#define DEVI __device__ __forceinline__

#define OUT_MODE 0
#define NT_X
#define NT_P
constexpr int L = 2048, D = 576, NM = 128, F = 4608;

constexpr size_t WS_UX   = 0;
constexpr size_t WS_HT   = WS_UX + (size_t)D * L * 4;
constexpr int    KREV_PITCH = 4352;
constexpr size_t WS_KREV = WS_HT + (size_t)D * L * 2;
constexpr size_t WS_WT   = WS_KREV + (size_t)D * KREV_PITCH;
constexpr size_t WT_BYTES = (size_t)288 * 36 * 1024;
constexpr size_t WS_W2T  = WS_WT + WT_BYTES + 65536;
constexpr size_t W2T_BYTES = (size_t)4 * 144 * 9 * 1024;
constexpr size_t WS_CSP  = WS_W2T + W2T_BYTES + 65536;
constexpr size_t WS_HFF  = WS_CSP + (size_t)32 * 4 * 512 * 4;
constexpr size_t HFF_BYTES = (size_t)128 * 144 * 1024;
constexpr size_t WS_PART = WS_HFF + HFF_BYTES;
constexpr size_t PART_BYTES = (size_t)64 * 4 * 4 * 9216;
constexpr size_t WS_FLAG = WS_PART + PART_BYTES;
constexpr size_t FLAG_BYTES = (size_t)512 * 128;
constexpr size_t WS_STAT = WS_FLAG + FLAG_BYTES;
constexpr size_t STAT_BYTES = (size_t)8 * 18 * 256 * 8;
constexpr size_t WS_SCR = WS_STAT + STAT_BYTES;

DEVI float bf2f(u32 v) { return __uint_as_float(v << 16); }
DEVI float bflo(u32 v) { return __uint_as_float(v << 16); }
DEVI float bfhi(u32 v) { return __uint_as_float(v & 0xffff0000u); }
DEVI u32 pk2(float lo, float hi) {
  bf2 r = {(__bf16)lo, (__bf16)hi};
  return __builtin_bit_cast(u32, r);
}
DEVI float rbf(float x) { return __uint_as_float(((u32)__builtin_bit_cast(u16, (__bf16)x)) << 16); }
DEVI void dma16(const char* sbase, u32 voff, u32 lds) {
  asm volatile("s_mov_b32 m0, %2\n\ts_nop 0\n\tglobal_load_lds_dwordx4 %0, %1 sc1" ::"v"(voff), "s"(sbase), "s"(lds) : "memory");
}
#define OPAQUE(x) asm volatile("" : "+v"(x))
#define PIN() __builtin_amdgcn_sched_barrier(0)
DEVI s4 tr_read(LDS_AS void* p) { return __builtin_amdgcn_ds_read_tr16_b64_v4i16((LDS_AS s4*)p); }
DEVI u32x4 tr_read2(LDS_AS char* p0, LDS_AS char* p1) {
  u32x2 a = __builtin_bit_cast(u32x2, tr_read(p0)), b = __builtin_bit_cast(u32x2, tr_read(p1));
  u32x4 r = {a.x, a.y, b.x, b.y};
  return r;
}
DEVI void st16_wt(__amdgpu_buffer_rsrc_t rs, u32 off, u32x4 v) { __builtin_amdgcn_raw_buffer_store_b128(v, rs, off, 0, 16); }
DEVI __amdgpu_buffer_rsrc_t mkrsrc(const void* p, u32 bytes) {
  return __builtin_amdgcn_make_buffer_rsrc((void*)p, 0, bytes, 0x00020000);
}
#define WAITV(n) asm volatile("s_waitcnt vmcnt(" #n ")" ::: "memory")
#define WAITL(n) asm volatile("s_waitcnt lgkmcnt(" #n ")" ::: "memory")
#define BAR() __builtin_amdgcn_s_barrier()
DEVI f32x16 mfma32(u32x4 a, u32x4 b, f32x16 c) {
  return __builtin_amdgcn_mfma_f32_32x32x16_bf16(__builtin_bit_cast(bf8, a), __builtin_bit_cast(bf8, b), c, 0, 0, 0);
}
DEVI f32x4 mfma16(u32x4 a, u32x4 b, f32x4 c) {
  return __builtin_amdgcn_mfma_f32_16x16x32_bf16(__builtin_bit_cast(bf8, a), __builtin_bit_cast(bf8, b), c, 0, 0, 0);
}
typedef _Float16 h2v __attribute__((ext_vector_type(2)));
typedef _Float16 h8v __attribute__((ext_vector_type(8)));
DEVI u32 pkh2(float lo, float hi) { h2v r = {(_Float16)lo, (_Float16)hi}; return __builtin_bit_cast(u32, r); }
DEVI f32x4 mfma16h(u32x4 a, u32x4 b, f32x4 c) {
  return __builtin_amdgcn_mfma_f32_16x16x32_f16(__builtin_bit_cast(h8v, a), __builtin_bit_cast(h8v, b), c, 0, 0, 0);
}
template <int CTRL> DEVI float dpp(float v) {
  return __builtin_bit_cast(float, __builtin_amdgcn_update_dpp(0, __builtin_bit_cast(int, v), CTRL, 0xf, 0xf, true));
}
DEVI float sum8(float v) {
  v += dpp<0xB1>(v); v += dpp<0x4E>(v); v += dpp<0x141>(v);
  return v;
}
DEVI float dot2(u32 a, u32 b, float c) {
  return __builtin_amdgcn_fdot2_f32_bf16(__builtin_bit_cast(bf2, a), __builtin_bit_cast(bf2, b), c, false);
}

#define TS_DECL
#define TS(idx)
#define TS_OUT(kern)

#define K1ORD 1
constexpr int K1_KG = 576, K1_LN = 128, K1_PT = 176;
constexpr int K1_LDS = 38 * 1024;

struct K1Args {
  const u16 *x, *Are, *Aim, *Bre, *Bim, *Cre, *Cim, *ldt, *w1, *w3, *w2;
  char* ws;
};

constexpr int lag_index(int l) {
  if (l < 256) return l;
  int e = 0;
  while ((l >> e) >= 256) e++;
  int q = l >> e;
  const int rem = l & ((1 << e) - 1), half = 1 << (e - 1);
  if (rem > half || (rem == half && (q & 1))) q++;
  return 128 * e + q;
}
constexpr int kg_p1(int r) { return r < 8 ? 256 + 2 * r : 512 + 256 * ((r - 8) >> 2) + 4 * ((r - 8) & 3); }
constexpr int kg_p2(int r) { return r < 8 ? 1024 + 256 * (r >> 1) + 8 * (r & 1) : 2048; }
struct KgTab {
  u16 g[256][8]; u16 t[256][2];
  constexpr KgTab() : g(), t() {
    for (int i = 0; i < 256; i++) {
      for (int j = 0; j < 8; j++) g[i][j] = (u16)(4 * lag_index(2047 - (i * 8 + j)));
      const int r = i & 15, c16 = (i >> 4) * 16;
      t[i][0] = (u16)(4 * lag_index(kg_p1(r) + c16));
      t[i][1] = (u16)(4 * ((r < 8 || i == 8) ? lag_index(kg_p2(r) + c16) : 700));
    }
  }
};
__constant__ __attribute__((aligned(16))) const KgTab kg_tab{};

DEVI int panel_log(int ft, int p) { const int ls = 4 - (ft & 3); return p < ls ? p : (p < ls + 4 ? p - ls + 5 : p - 4); }
DEVI int panel_phys(int ft, int lp) { const int ls = 4 - (ft & 3); return lp < ls ? lp : (lp < 5 ? lp + 4 : lp - 5 + ls); }

template <int NT, int NV>
DEVI void retile_addr(const u16* w1, const u16* w3, const u16* w2, int unit, int tid, const u32x4* (&p)[NV]) {
  if (unit < 288) {
    const int fq = unit >> 2, kq = unit & 3, ch = tid & 15;
    const u16* src = ((ch >> 3) ? w3 : w1) + (size_t)(kq * 144) * F + fq * 64 + (ch & 7) * 8;
#pragma unroll
    for (int i = 0; i < NV; i++) {
      int k = (tid + NT * i) >> 4; k = k > 143 ? 143 : k;
      p[i] = (const u32x4*)(src + (size_t)k * F);
    }
  } else {
    const int sg = unit - 288, ks = sg / 36, q = sg - ks * 36;
    const int fa = ks * 8 + (q < 32 ? (q & 7) : 2 * (q - 32)), fb = q < 32 ? fa : fa + 1;
    const int Pa = fa * 9 + panel_phys(fa, q < 32 ? 2 * (q >> 3) : 8), Pb = fb * 9 + panel_phys(fb, q < 32 ? 2 * (q >> 3) + 1 : 8);
    const u32x4* srcA = (const u32x4*)(w2 + (size_t)Pa * 16 * D);
    const u32x4* srcB = (const u32x4*)(w2 + (size_t)Pb * 16 * D) - 1152;
#pragma unroll
    for (int i = 0; i < NV; i++) {
      int c = tid + NT * i; c = c > 2303 ? 2303 : c;
      p[i] = (c < 1152 ? srcA : srcB) + c;
    }
  }
}
template <int NT, int NV>
DEVI void retile_load(const u16* w1, const u16* w3, const u16* w2, int unit, int tid, u32x4 (&v)[NV]) {
  const u32x4* p[NV];
  retile_addr<NT, NV>(w1, w3, w2, unit, tid, p);
#pragma unroll
  for (int i = 0; i < NV; i++) v[i] = __builtin_nontemporal_load(p[i]);
}
template <int NT, int NV>
DEVI void retile_stage(LDS_AS char* smem, int unit, int tid, const u32x4 (&v)[NV]) {
  if (unit < 288) {
    const int ch = tid & 15;
#pragma unroll
    for (int i = 0; i < NV; i++) {
      int k = (tid + NT * i) >> 4; k = k > 143 ? 143 : k;
      *(LDS_AS u32x4*)(smem + k * 256 + (((ch >> 1) ^ (k & 3)) * 32) + (ch & 1) * 16) = v[i];
    }
  } else {
#pragma unroll
    for (int i = 0; i < NV; i++) {
      int c = tid + NT * i; c = c > 2303 ? 2303 : c;
      const int r = c / 72, cc = c - r * 72;
      *(LDS_AS u32x4*)(smem + r * 1152 + ((cc ^ (r & 6)) * 16)) = v[i];
    }
  }
}
struct RtDst { const char* base; u32 voff; int k0; };
DEVI u32 rt_off(const RtDst& d, int i) { return d.k0 < 0 ? i * 1024 : ((d.k0 + i) >> 1) * 2048 + ((d.k0 + i) & 1) * 512; }
DEVI RtDst retile_read(char* ws, LDS_AS char* smem, int unit, int lane, int wid, bool pub, u32x4 (&fr)[9]) {
  const int q4 = (lane & 15) >> 2, p4 = lane & 3;
  RtDst d;
  if (unit < 288) {
    const int fq = unit >> 2, kq = unit & 3;
    const int P = fq * 4 + wid, ft = P / 9, lp = panel_log(ft, P - ft * 9);
    const int mat = lane >> 5, g2 = (lane >> 4) & 1;
    float cs = 0.f;
#pragma unroll
    for (int kk = 0; kk < 9; kk++) {
      LDS_AS char* p0 = smem + (kk * 16 + 8 * g2 + q4) * 256 + (((mat * 4 + wid) ^ q4) * 32) + p4 * 8;
      fr[kk] = tr_read2(p0, p0 + 4 * 256);
#pragma unroll
      for (int j = 0; j < 4; j++) cs = dot2(fr[kk][j], 0x3f803f80u, cs);
    }
    cs += __shfl_xor(cs, 16);
    if (pub && g2 == 0) {
      float* csp = (float*)(ws + WS_CSP) + ((size_t)ft * 512 + lp * 32 + mat * 16 + (lane & 15)) * 4 + kq;
      __hip_atomic_store(csp, cs, __ATOMIC_RELAXED, __HIP_MEMORY_SCOPE_AGENT);
    }
    d.base = ws + WS_WT + (size_t)(ft * 9 + lp) * 36864; d.voff = mat * 1024 + (lane & 31) * 16; d.k0 = kq * 9;
  } else {
    const int sg = unit - 288;
    const int row0 = 8 * (lane >> 4) + q4;
#pragma unroll
    for (int nf = 0; nf < 9; nf++) {
      LDS_AS char* p0 = smem + row0 * 1152 + (((wid * 9 + nf) ^ ((row0 >> 1) & 3)) * 32) + p4 * 8;
      LDS_AS char* p1 = smem + (row0 + 4) * 1152 + (((wid * 9 + nf) ^ (((row0 + 4) >> 1) & 3)) * 32) + p4 * 8;
      fr[nf] = tr_read2(p0, p1);
    }
    d.base = ws + WS_W2T + (size_t)(wid * 144 + sg) * 9216; d.voff = lane * 16; d.k0 = -1;
  }
  return d;
}

__global__ __launch_bounds__(256, 4) void k1_pre(K1Args a) {
  extern __shared__ __attribute__((aligned(16))) char smem_[];
  LDS_AS char* smem = (LDS_AS char*)smem_;
  const int tid = threadIdx.x, lane = tid & 63;
  const int wid = __builtin_amdgcn_readfirstlane(tid >> 6);
  int b = blockIdx.x;
  TS_DECL; TS(0);
  if (b < K1_KG) {
    const int d = b;
    LDS_AS f32x4* prm = (LDS_AS f32x4*)(smem + 16384);
    if (tid < NM) {
      const int n = d * NM + tid;
#define LDP(p) bf2f(__builtin_nontemporal_load(p + n))
      const float Are = LDP(a.Are), Aim = LDP(a.Aim), Bre = LDP(a.Bre), Bim = LDP(a.Bim);
      const float cr = LDP(a.Cre), ci = LDP(a.Cim);
      const float dt = rbf(__expf(bf2f(a.ldt[d])));
      const float br = dt * Bre, bi = dt * Bim;
      f32x4 v = {dt * Are * 1.44269504088896341f,
                 dt * Aim * 0.15915494309189535f,
                 cr * br - ci * bi, cr * bi + ci * br};
      prm[tid] = v;
    }
    const int r = lane & 15, kq = lane >> 4;
    const float p0 = (float)r, p1 = (float)kg_p1(r), p2 = (float)kg_p2(r), pY = (float)(16 * r);
    const u32x4 gi = *(const u32x4*)kg_tab.g[tid];
    const u32 ti = *(const u32*)kg_tab.t[tid];
    WAITL(0); BAR(); TS(3);
    f32x4 acc0 = {0, 0, 0, 0}, acc1 = acc0, acc2 = acc0;
    const f32x4 qm = prm[32 * wid + (lane & 31)];
    if (__all(qm[0] <= 0.f && fabsf(qm[1]) <= 0.125f)) {
      float cm = fabsf(qm[2]) + fabsf(qm[3]);
      cm = fmaxf(cm, dpp<0xB1>(cm)); cm = fmaxf(cm, dpp<0x4E>(cm)); cm = fmaxf(cm, dpp<0x141>(cm)); cm = fmaxf(cm, dpp<0x140>(cm));
      const float cmax = fmaxf(__builtin_bit_cast(float, __builtin_amdgcn_readlane(__builtin_bit_cast(int, cm), 0)),
                               __builtin_bit_cast(float, __builtin_amdgcn_readlane(__builtin_bit_cast(int, cm), 16)));
      const int E = __builtin_amdgcn_frexp_expf(cmax);
      const float ey = (float)(14 - E);
      const float oscale = __builtin_amdgcn_ldexpf(1.0f, E - 28);
#pragma unroll
      for (int s = 0; s < 2; s++) {
        u32x4 x0, x1, x2, y;
#pragma unroll
        for (int m4 = 0; m4 < 4; m4++) {
          const f32x4 q = prm[32 * wid + 16 * s + 4 * kq + m4];
          const f32x2 q01 = {q[0], q[1]};
          auto zpow = [&](float p, float sc) {
            const f32x2 pp = {p, p}, ss = {sc, 0.f};
            const f32x2 ar = __builtin_elementwise_fma(q01, pp, ss);
            const float e = __builtin_amdgcn_exp2f(ar[0]);
            const f32x2 cs = {__builtin_amdgcn_cosf(ar[1]), __builtin_amdgcn_sinf(ar[1])}, ee = {e, e};
            return cs * ee;
          };
          const f32x2 yv = zpow(pY, ey);
          y[m4] = pkh2(q[2] * yv[0] - q[3] * yv[1], -(q[2] * yv[1] + q[3] * yv[0]));
          f32x2 v;
          v = zpow(p0, 14.f); x0[m4] = pkh2(v[0], v[1]);
          v = zpow(p1, 14.f); x1[m4] = pkh2(v[0], v[1]);
          v = zpow(p2, 14.f); x2[m4] = pkh2(v[0], v[1]);
        }
        acc0 = mfma16h(x0, y, acc0); acc1 = mfma16h(x1, y, acc1); acc2 = mfma16h(x2, y, acc2);
      }
      acc0 *= oscale; acc1 *= oscale; acc2 *= oscale;
    } else {
#pragma unroll 1
      for (int s = 0; s < 2; s++) {
        u32x4 xh0, xl0, xh1, xl1, xh2, xl2, yh, yl;
#pragma unroll
        for (int m4 = 0; m4 < 4; m4++) {
          const f32x4 q = prm[32 * wid + 16 * s + 4 * kq + m4];
          auto zpow = [&](float p, float& re, float& im) {
            float e = __builtin_amdgcn_exp2f(q[0] * p);
            float ph = __builtin_amdgcn_fractf(q[1] * p);
            re = e * __builtin_amdgcn_cosf(ph); im = e * __builtin_amdgcn_sinf(ph);
          };
          auto split = [&](float re, float im, u32& hi, u32& lo) {
            hi = pk2(re, im);
            lo = pk2(re - bflo(hi), im - bfhi(hi));
          };
          float yr, yi, re, im; u32 h, l;
          zpow(pY, yr, yi);
          split(q[2] * yr - q[3] * yi, -(q[2] * yi + q[3] * yr), h, l); yh[m4] = h; yl[m4] = l;
          zpow(p0, re, im); split(re, im, h, l); xh0[m4] = h; xl0[m4] = l;
          zpow(p1, re, im); split(re, im, h, l); xh1[m4] = h; xl1[m4] = l;
          zpow(p2, re, im); split(re, im, h, l); xh2[m4] = h; xl2[m4] = l;
        }
        acc0 = mfma16(xl0, yh, acc0); acc1 = mfma16(xl1, yh, acc1); acc2 = mfma16(xl2, yh, acc2);
        acc0 = mfma16(xh0, yl, acc0); acc1 = mfma16(xh1, yl, acc1); acc2 = mfma16(xh2, yl, acc2);
        acc0 = mfma16(xh0, yh, acc0); acc1 = mfma16(xh1, yh, acc1); acc2 = mfma16(xh2, yh, acc2);
      }
    }
    LDS_AS float* part = (LDS_AS float*)smem;
    *(LDS_AS f32x4*)(part + ((wid * 3 + 0) * 16 + r) * 16 + 4 * kq) = acc0;
    *(LDS_AS f32x4*)(part + ((wid * 3 + 1) * 16 + r) * 16 + 4 * kq) = acc1;
    *(LDS_AS f32x4*)(part + ((wid * 3 + 2) * 16 + r) * 16 + 4 * kq) = acc2;
    __syncthreads(); TS(4);
    LDS_AS float* tab = part + 4 * 768;
    LDS_AS char* tb = (LDS_AS char*)tab;
    tab[tid] = (part[tid] + part[768 + tid]) + (part[2 * 768 + tid] + part[3 * 768 + tid]);
    *(LDS_AS float*)(tb + (ti & 0xffffu)) = (part[256 + tid] + part[768 + 256 + tid]) + (part[2 * 768 + 256 + tid] + part[3 * 768 + 256 + tid]);
    *(LDS_AS float*)(tb + (ti >> 16)) = (part[512 + tid] + part[768 + 512 + tid]) + (part[2 * 768 + 512 + tid] + part[3 * 768 + 512 + tid]);
    __syncthreads(); TS(5);
    auto rsk = mkrsrc(a.ws + WS_KREV + (size_t)d * KREV_PITCH, KREV_PITCH);
    u32x4 o;
#pragma unroll
    for (int j = 0; j < 4; j++) o[j] = pk2(*(LDS_AS float*)(tb + (gi[j] & 0xffffu)), *(LDS_AS float*)(tb + (gi[j] >> 16)));
    st16_wt(rsk, tid * 16, o);
    if (tid < 16) { u32x4 z = {0, 0, 0, 0}; st16_wt(rsk, 4096 + tid * 16, z); }
    else if (tid < 48) { u32x4 z = {~0u, ~0u, ~0u, ~0u}; st16_wt(mkrsrc(a.ws + WS_STAT, (u32)STAT_BYTES), (d * 32 + tid - 16) * 16, z); }
    else if (tid < 56 && d < 512) { u32x4 z = {0, 0, 0, 0}; st16_wt(mkrsrc(a.ws + WS_FLAG, (u32)FLAG_BYTES), (d * 8 + tid - 48) * 16, z); }
  } else if (b < K1_KG + K1_LN) {
    __builtin_amdgcn_s_setprio(3);
    const int lb = b - K1_KG;
    const int rb = (lb >> 4) * 8 + (lb & 7), chalf = (lb >> 3) & 1;
    constexpr int PITCH = 1152 + 32;
    const u32x4* src = (const u32x4*)(a.x + (size_t)rb * 32 * D);
    u32x4 v[9];
#pragma unroll
    for (int i = 0; i < 9; i++) v[i] = __builtin_nontemporal_load(src + tid + 256 * i);
#pragma unroll
    for (int i = 0; i < 9; i++) {
      int c = tid + 256 * i, r = c / 72, cc = c - r * 72;
      *(LDS_AS u32x4*)(smem + r * PITCH + cc * 16) = v[i];
    }
    __syncthreads(); TS(3);
    const int r = tid >> 3, sub = tid & 7;
    u32x4 q[9];
    f32x4 s4v = {0, 0, 0, 0};
#pragma unroll
    for (int i = 0; i < 9; i++) {
      q[i] = *(LDS_AS u32x4*)(smem + r * PITCH + (sub + 8 * i) * 16);
#pragma unroll
      for (int j = 0; j < 4; j++) s4v[j] = dot2(q[i][j], 0x3f803f80u, s4v[j]);
    }
    const float mean = sum8((s4v[0] + s4v[1]) + (s4v[2] + s4v[3])) * (1.0f / D);
    f32x4 qa = {0, 0, 0, 0}, qb = qa;
#pragma unroll
    for (int i = 0; i < 9; i++)
#pragma unroll
      for (int j = 0; j < 4; j++) {
        const float d0 = bflo(q[i][j]) - mean, d1 = bfhi(q[i][j]) - mean;
        qa[j] = fmaf(d0, d0, qa[j]); qb[j] = fmaf(d1, d1, qb[j]);
      }
    qa += qb;
    const float q2 = sum8((qa[0] + qa[1]) + (qa[2] + qa[3]));
    LDS_AS f32x2* st = (LDS_AS f32x2*)(smem + 32 * PITCH);
    if (sub == 0) {
      const float sd = rbf(sqrtf(rbf(rbf(q2 * (1.0f / D)) + 1e-5f)));
      f32x2 o = {rbf(mean), 1.0f / sd};
      st[r] = o;
    }
    __syncthreads(); TS(4);
    const int g = lane >> 4, i16 = lane & 15, q4 = i16 >> 2, p4 = i16 & 3;
    f32x2 rst[8];
#pragma unroll
    for (int j = 0; j < 8; j++) rst[j] = st[8 * g + j];
    auto rsu = mkrsrc(a.ws + WS_UX + (size_t)rb * D * 128, D * 128);
#pragma unroll
    for (int it = 0; it < 5; it++) {
      if (wid + 4 * it >= 18) break;
      const int cg = chalf * 18 + wid + 4 * it;
      LDS_AS char* p0 = smem + (8 * g + q4) * PITCH + cg * 32 + p4 * 8;
      u32x4 xv = tr_read2(p0, p0 + 4 * PITCH);
      u32x4 uv;
#pragma unroll
      for (int j = 0; j < 4; j++) {
        const u32 dpk = pk2(bflo(xv[j]) - rst[2 * j][0], bfhi(xv[j]) - rst[2 * j + 1][0]);
        uv[j] = pk2(bflo(dpk) * rst[2 * j][1], bfhi(dpk) * rst[2 * j + 1][1]);
      }
      const u32 off = (u32)(cg * 16 + i16) * 128 + g * 16;
      st16_wt(rsu, off, uv);
      st16_wt(rsu, off + 64, xv);
    }
  } else {
    __builtin_amdgcn_s_setprio(2);
#define PT_DELAY 3
    const int pb = b - K1_LN - K1_KG;
    u32 cpf = 0;
    if (pb < 40 && wid == 0) {
      u64 pc;
      asm volatile("s_getpc_b64 s[40:41]\n\ts_add_u32 s40, s40, _Z7k2_main6K2Args@rel32@lo+4\n\ts_addc_u32 s41, s41, _Z7k2_main6K2Args@rel32@hi+12\n\ts_mov_b64 %0, s[40:41]"
                   : "=s"(pc) :: "s40", "s41", "scc");
      cpf = *(const volatile u32*)((const char*)pc + (pb >> 3) * 8192 + lane * 128);
    }
    for (int i = 0; i < PT_DELAY; i++) __builtin_amdgcn_s_sleep(8);
    asm volatile("" ::"v"(cpf));
    const int qi = pb >> 2;
    const int unit = pb < 160 ? (((qi / 5) * 9 + 2 * (qi % 5)) << 2) + (pb & 3) : 288 + 128 + (pb - 160);
    u32x4 v[9];
    retile_load<256, 9>(a.w1, a.w3, a.w2, unit, tid, v);
    retile_stage<256, 9>(smem, unit, tid, v);
    __syncthreads();
    const RtDst rd = retile_read(a.ws, smem, unit, lane, wid, true, v);
    const auto rsw = mkrsrc(rd.base, 36864);
#pragma unroll
    for (int i = 0; i < 9; i++) st16_wt(rsw, rt_off(rd, i) + rd.voff, v[i]);
  }
  TS(1);
  TS(2); TS_OUT(0);
}

constexpr int CV_CP = 4160;
constexpr int CV_UOFF = 4 * CV_CP;
constexpr int CV_BYTES = CV_UOFF + 4608;
constexpr int CV_K0 = 36864;
constexpr int RA = 0, RB = 36864, RC = 73728, RD = 110592;
constexpr int G1_RING = 36864, G1_ACT = 16384 + 128, G1_STAGE = G1_ACT + 4096;
constexpr int G1_RT = G1_RING + 4 * G1_STAGE;
constexpr int G1_CSOFF = G1_RING + 6 * G1_STAGE;
constexpr int G2_RES = 147456;
constexpr int K2_LDS = G1_CSOFF + 2048;
constexpr int g2_slot(int s) { return (s & 3) == 0 ? RD : ((s & 3) == 1 ? RA : ((s & 3) == 2 ? RB : RC)); }
constexpr int g1_region(int p) { return p == 0 || p == 6 ? RA : (p == 3 || p == 7 ? RB : (p == 4 || p == 8 ? RC : RD)); }
constexpr int FL_HFF = 32768, FL_A = 57344, FL_B = 59392, FL_RT = 61440, FL_IN = 63488;

struct K2Args { const u16 *dskip, *w1, *w3, *w2; char* ws; u16* out; };

struct G1Acc { f32x4 v[2][2]; };
struct G1Row { float mu[2], sd2[2], nrl2[2]; };
template <int PANEL>
DEVI void g1_epilogue(const G1Acc& acc, LDS_AS char* smem, int g, const G1Row& r, __amdgpu_buffer_rsrc_t rsh, u32 voff, int ft) {
  LDS_AS float* cs = (LDS_AS float*)(smem + G1_CSOFF) + PANEL * 32 + 4 * g;
  const f32x4 cg = *(LDS_AS f32x4*)cs, cu = *(LDS_AS f32x4*)(cs + 16);
  float o[2][4];
#pragma unroll
  for (int mf = 0; mf < 2; mf++) {
#pragma unroll
    for (int j = 0; j < 4; j++) {
      float tg = fmaf(-r.mu[mf], cg[j], acc.v[0][mf][j]);
      asm("" : "+v"(tg));
      float tu = fmaf(-r.mu[mf], cu[j], acc.v[1][mf][j]);
      asm("" : "+v"(tu));
      const float e = __builtin_amdgcn_exp2f(tg * r.nrl2[mf]);
      const float q = __builtin_amdgcn_rcpf(fmaf(e, r.sd2[mf], r.sd2[mf]));
      float p = tg * tu;
      asm("" : "+v"(p));
      p *= q;
      asm("" : "+v"(p));
      o[mf][j] = p;
    }
  }
  const auto s0 = __builtin_amdgcn_permlane16_swap(pk2(o[0][0], o[0][1]), pk2(o[1][0], o[1][1]), false, false);
  const auto s1 = __builtin_amdgcn_permlane16_swap(pk2(o[0][2], o[0][3]), pk2(o[1][2], o[1][3]), false, false);
  const u32x4 v = {s0[0], s1[0], s0[1], s1[1]};
  const int ftl = ft & 7;
  const int q = PANEL < 8 ? (PANEL >> 1) * 8 + ftl : 32 + (ftl >> 1);
  const int half = PANEL < 8 ? (PANEL & 1) : (ftl & 1);
  st16_wt(rsh, voff + (u32)((ft >> 3) * 36 + q) * 1024 + (u32)half * 512, v);
}

struct G1Bases { LDS_AS char* b[3]; };
template <int OFF>
DEVI u32x4 g1_ldw(const G1Bases& wb) { constexpr int I = OFF / 49152 < 2 ? OFF / 49152 : 2; return *(LDS_AS u32x4*)(wb.b[I] + (OFF - I * 49152)); }

struct G1Ctx {
  const char *hTw, *Wt, *Wq;
  u32 aoff, loff;
  u32 lds0, ldsw, ldsa;
  u32 rot2k;
  int wid;
  G1Bases wb, ab;
  const char *asrc, *wsrc;
};
template <int I>
DEVI void g2_piece(const G1Ctx& c, int s, u32 slot) {
  if constexpr (I < 4) dma16(c.asrc + (size_t)(I >> 1) * 144 * 1024 + s * 2048 + (I & 1) * 1024, c.loff, c.lds0 + slot + c.wid * 4096 + I * 1024);
  else if constexpr (I < 8) dma16(c.wsrc + (size_t)s * 18432 + (I - 4) * 4096, c.loff, c.ldsw + slot + 16384 + (I - 4) * 4096);
  else { if (c.wid < 2) dma16(c.wsrc + (size_t)s * 18432 + 16384, c.loff, c.ldsw + slot + 32768); }
}
DEVI void g2_stage_dma(const G1Ctx& c, int s, u32 slot) {
  g2_piece<0>(c, s, slot); g2_piece<1>(c, s, slot); g2_piece<2>(c, s, slot); g2_piece<3>(c, s, slot); g2_piece<4>(c, s, slot);
  g2_piece<5>(c, s, slot); g2_piece<6>(c, s, slot); g2_piece<7>(c, s, slot); g2_piece<8>(c, s, slot);
}
DEVI u32 g1_rot(const G1Ctx& c, u32 off) { const u32 x = off + c.rot2k; return x >= 36864u ? x - 36864u : x; }
template <int S, int I>
DEVI void g1_piece(const G1Ctx& c) {
  const u32 po = g1_rot(c, S * 2048);
  constexpr u32 dst = G1_RING + (S % 6) * G1_STAGE;
  if constexpr (I < 2) dma16(c.hTw + I * 16 * (L * 2), c.aoff + po * 64u, c.ldsa + dst + I * 8256);
  else { if ((c.wid >> 2) == (S & 1)) dma16(c.Wq, c.loff + po, c.ldsw + dst + G1_ACT - (S & 1) * 4096); }
}
template <int S> DEVI void g1_stage3(const G1Ctx& c) { g1_piece<S, 0>(c); g1_piece<S, 1>(c); g1_piece<S, 2>(c); }
template <int P>
DEVI void g1_frag(const G1Ctx& c, int fr) {
  fr = fr > 35 ? 35 : fr;
  dma16(c.Wt + P * 36864, c.loff + g1_rot(c, fr * 1024), c.lds0 + g1_region(P) + fr * 1024);
}
template <int PANEL, int I>
DEVI void g1_refill(const G1Ctx& c) {
  if constexpr (PANEL == 3) { if constexpr (I < 2) g1_frag<5>(c, 24 + 8 * I + c.wid); else if constexpr (I < 7) g1_frag<6>(c, 8 * (I - 2) + c.wid); }
  else if constexpr (PANEL < 6) {
    if constexpr (I < 5) g1_frag<PANEL + 3>(c, 8 * I + c.wid);
  }
  else { if (c.wid < 4) g2_piece<I>(c, PANEL - 6, g2_slot(PANEL - 6)); }
}

template <int PANEL, int K>
struct G1PassStep {
  static DEVI void run(G1Acc& acc, u32x4 (&w)[3], const u32x4 (&act)[36], const G1Ctx& c) {
    u32x4 cur = w[K % 3];
    acc.v[K & 1][0] = mfma16(cur, act[K & ~1], acc.v[K & 1][0]);
    acc.v[K & 1][1] = mfma16(cur, act[K | 1], acc.v[K & 1][1]);
    OPAQUE(acc.v[K & 1][0]); OPAQUE(acc.v[K & 1][1]);
    if constexpr (K + 3 < 36) w[K % 3] = g1_ldw<g1_region(PANEL) + (K + 3) * 1024>(c.wb);
    if constexpr (K % 4 == 1) g1_refill<PANEL, K / 4>(c);
    PIN();
    if constexpr (K + 1 < 36) G1PassStep<PANEL, K + 1>::run(acc, w, act, c);
  }
};
template <int PANEL>
DEVI G1Acc g1_pass(const u32x4 (&act)[36], const G1Ctx& c) {
  G1Acc acc = {};
  u32x4 w[3];
  constexpr int R = g1_region(PANEL);
  w[0] = g1_ldw<R>(c.wb); w[1] = g1_ldw<R + 1024>(c.wb); w[2] = g1_ldw<R + 2048>(c.wb);
  PIN();
  G1PassStep<PANEL, 0>::run(acc, w, act, c);
  return acc;
}

template <int J>
DEVI u32x4 g1_lda(const G1Ctx& c) {
  constexpr int OFF = G1_RING + ((J >> 1) % 6) * G1_STAGE + (J & 1) * 32, I = OFF / 49152 < 2 ? OFF / 49152 : 2;
  LDS_AS char* p = c.ab.b[I] + (OFF - I * 49152);
  return tr_read2(p, p + 2048);
}
template <int J, int P>
DEVI u32x4 g1_ldw1(const G1Ctx& c) {
  if constexpr (P == 0) return g1_ldw<RA + J * 1024>(c.wb);
  else return g1_ldw<G1_RING + ((J >> 1) % 6) * G1_STAGE + G1_ACT + (P - 1) * 2048 + (J & 1) * 1024>(c.wb);
}
DEVI void flags_add(const char* fl, int b) { __hip_atomic_fetch_add((u32*)(fl + (b & 15) * 128), 1u, __ATOMIC_RELAXED, __HIP_MEMORY_SCOPE_AGENT); }
DEVI bool flags_set(u32 v) { return __all(v >= 16u); }
DEVI void flags_spin(const char* fl, u32 foff) {
  for (u32 spins = 0; spins < (1u << 22); ++spins) {
    u32 v;
    asm volatile("global_load_dword %0, %1, %2 sc1\n\ts_waitcnt vmcnt(0)" : "=&v"(v) : "v"(foff), "s"(fl) : "memory");
    if (flags_set(v)) break;
    __builtin_amdgcn_s_sleep(1);
  }
}

template <int N> DEVI void waitv() { asm volatile("s_waitcnt vmcnt(%0)" ::"i"(N) : "memory"); }

struct G1Stat {
  const char *stat, *flag, *csp;
  u32 soff, coff, foff; int ft, b;
  u32x2 g[3]; u32 fl; u32x4 cs;
  RtDst rd;
};

template <int J>
struct G1P1Step {
  static DEVI void run(const G1Ctx& c, G1Acc (&acc)[4], u32x4 (&w)[3], u32x4 (&act)[36], G1Stat& st, const u32x4 (&rf)[9], int lane) {
    constexpr int S = J >> 1;
    if constexpr (J & 1) {
      if constexpr (S < 3) { if (c.wid >= 4) waitv<(S == 0 ? 15 : (S == 1 ? 10 : 7))>(); }
      else if constexpr (S <= 8) { if (c.wid >= 4) waitv<6>(); else waitv<9>(); }
      else if constexpr (S <= 13) waitv<6>();
      else if constexpr (S < 17) waitv<S - 4>();
      else waitv<5>();
      BAR();
      if constexpr (S == 11) { if (c.wid == 0 && lane == 0) flags_add(st.flag + FL_RT, st.b); }
      if constexpr (S == 13) {
#pragma unroll
        for (int i = 0; i < 3; i++)
          asm volatile("global_load_dwordx2 %0, %1, %2 sc1" : "=v"(st.g[i]) : "v"(st.soff), "s"(st.stat + i * 2048) : "memory");
        asm volatile("global_load_dwordx4 %0, %1, %2 sc1" : "=v"(st.cs) : "v"(st.coff), "s"(st.csp) : "memory");
      }
      if constexpr (S == 15) asm volatile("global_load_dword %0, %1, %2 sc1" : "=v"(st.fl) : "v"(st.foff), "s"(st.flag + FL_RT) : "memory");
      if constexpr (S == 17) {
        asm volatile("" : "+v"(st.fl));
        if (!flags_set(st.fl)) flags_spin(st.flag + FL_RT, st.foff);
      }
    }
    if constexpr ((J & 1) && J + 1 < 36) { act[J + 1] = g1_lda<J + 1>(c); act[J + 2] = g1_lda<J + 2>(c); }
    PIN();
#define G1_ONE(P)                                              \
    { u32x4 cur = w[P];                                        \
      acc[P].v[J & 1][0] = mfma16(cur, act[J & ~1], acc[P].v[J & 1][0]);   \
      acc[P].v[J & 1][1] = mfma16(cur, act[J | 1], acc[P].v[J & 1][1]);    \
      OPAQUE(acc[P].v[J & 1][0]); OPAQUE(acc[P].v[J & 1][1]);  \
      if constexpr (J + 1 < 36) w[P] = g1_ldw1<J + 1, P>(c);   \
      PIN(); }
    constexpr int TP = 3 + (S - 13) / 2, TF = S == 14 ? 16 : (S == 16 ? 24 : 0);
    constexpr bool T3 = S == 14 || S == 15 || S == 17;
    G1_ONE(0)
    if constexpr ((J & 1) && S > 0) { if constexpr (S < 13) g1_piece<S + 5, 0>(c); else g1_frag<TP>(c, TF + c.wid); PIN(); }
    if constexpr (!(J & 1) && S < 9) {
      if (c.wid < 4) asm volatile("global_store_dwordx4 %0, %1, %2 sc1\n\ts_nop 1" ::"v"(st.rd.voff), "v"(rf[S]), "s"(st.rd.base + rt_off(st.rd, S)) : "memory");
      PIN();
    }
    if constexpr (J == 6) {
      if (st.ft < 18 && st.ft % 3 == 0) {
        float s1[2] = {0.f, 0.f}, s2[2] = {0.f, 0.f};
#pragma unroll
        for (int k = 0; k < 6; k++)
#pragma unroll
          for (int j = 0; j < 4; j++) { s1[k & 1] = dot2(act[k][j], 0x3f803f80u, s1[k & 1]); s2[k & 1] = dot2(act[k][j], act[k][j], s2[k & 1]); }
#pragma unroll
        for (int i = 0; i < 2; i++) {
          s1[i] += __shfl_xor(s1[i], 16); s1[i] += __shfl_xor(s1[i], 32);
          s2[i] += __shfl_xor(s2[i], 16); s2[i] += __shfl_xor(s2[i], 32);
        }
        u32x2 o = {__float_as_uint((lane & 16) ? s1[1] : s1[0]), __float_as_uint((lane & 16) ? s2[1] : s2[0])};
        if (o.y == ~0u) o.y = 0x7fc00000u;
        asm volatile("global_store_dwordx2 %0, %1, %2 sc1\n\ts_nop 1" ::"v"(st.soff & 2047u), "v"(o), "s"(st.stat + (st.ft / 3) * 2048) : "memory");
      }
      PIN();
    }
    G1_ONE(1)
    if constexpr (!(J & 1) && S - 1 > 0 && S - 1 < 13) { g1_piece<S + 4, 2>(c); PIN(); }
    if constexpr ((J & 1) && T3) { g1_frag<TP>(c, TF + 16 + c.wid); PIN(); }
    G1_ONE(2)
    if constexpr ((J & 1) && S > 0) { if constexpr (S < 13) g1_piece<S + 5, 1>(c); else g1_frag<TP>(c, TF + 8 + c.wid); PIN(); }
#undef G1_ONE
    if constexpr (J + 1 < 36) G1P1Step<J + 1>::run(c, acc, w, act, st, rf, lane);
  }
};

template <int SLOT_N, int KK_N, bool SYNC>
DEVI void g2_step(f32x4 (&acc)[2][9], const u32x4 (&A)[2], const u32x4 (&W)[9], u32x4 (&An)[2], u32x4 (&Wn)[9],
                  const G1Bases& ab, const G1Bases& wb) {
#define G2_MM(nf) acc[0][nf] = mfma16(W[nf], A[0], acc[0][nf]); acc[1][nf] = mfma16(W[nf], A[1], acc[1][nf]); \
                  OPAQUE(acc[0][nf]); OPAQUE(acc[1][nf]); PIN();
#define G2_LW(nf) Wn[nf] = g1_ldw<SLOT_N + 16384 + KK_N * 9216 + (nf) * 1024>(wb);
  G2_MM(0)
  An[0] = g1_ldw<SLOT_N + KK_N * 1024>(ab); An[1] = g1_ldw<SLOT_N + 2048 + KK_N * 1024>(ab); PIN();
  G2_MM(1) G2_LW(0) G2_LW(1) PIN();
  G2_MM(2) G2_LW(2) G2_LW(3) PIN();
  G2_MM(3) G2_LW(4) G2_LW(5) PIN();
  G2_MM(4) G2_LW(6) G2_LW(7) PIN();
  G2_MM(5) G2_LW(8) PIN();
  G2_MM(6)
  G2_MM(7)
  if constexpr (SYNC) { WAITL(0); BAR(); PIN(); }
  G2_MM(8)
#undef G2_MM
#undef G2_LW
}
template <int S4, bool LAST>
DEVI void g2_stage(f32x4 (&o)[2][9], u32x4 (&A0)[2], u32x4 (&W0)[9], u32x4 (&A1)[2], u32x4 (&W1)[9], const G1Bases& ab, const G1Bases& wb) {
  g2_step<g2_slot(S4), 1, !LAST>(o, A0, W0, A1, W1, ab, wb);
  g2_step<g2_slot(LAST ? S4 : S4 + 1), 0, false>(o, A1, W1, A0, W0, ab, wb);
}

template <int E0, int NE>
DEVI void cv_copies(LDS_AS char* base, int tt, const u32x4& k0, const u32x4& k1) {
  const u32 w[8] = {k0.x, k0.y, k0.z, k0.w, k1.x, k1.y, k1.z, k1.w};
#pragma unroll
  for (int e = E0; e < E0 + NE; e++) {
    u32x4 o;
#pragma unroll
    for (int j = 0; j < 4; j++)
      o[j] = (e & 1) ? __builtin_amdgcn_alignbyte(w[(e >> 1) + j + 1], w[(e >> 1) + j], 2) : w[(e >> 1) + j];
    *(LDS_AS u32x4*)(base + e * CV_CP + tt * 16) = o;
  }
}
DEVI u32x4 cv_lda(LDS_AS char* p) {
  const u32x2 lo = *(volatile LDS_AS u32x2*)p, hi = *(volatile LDS_AS u32x2*)(p + 8);
  return (u32x4){lo.x, lo.y, hi.x, hi.y};
}
template <int NW, int STRIDE>
DEVI void cv_sum(LDS_AS char* p, f32x4& y0, f32x4& y1) {
  y0 = (f32x4){0, 0, 0, 0}; y1 = y0;
#pragma unroll
  for (int w = 0; w < NW; w++) { y0 += *(LDS_AS f32x4*)(p + w * STRIDE); y1 += *(LDS_AS f32x4*)(p + w * STRIDE + 256); }
}
DEVI u32x4 cv_out(const f32x4& y0, const f32x4& y1, const u32x4& uv, const u32x4& xv, float dsk) {
  const float y[8] = {y0[0], y0[1], y0[2], y0[3], y1[0], y1[1], y1[2], y1[3]};
  u32x4 o;
#pragma unroll
  for (int j = 0; j < 4; j++) {
    const u32 yb = pk2(y[2 * j], y[2 * j + 1]), sk = pk2(bflo(uv[j]) * dsk, bfhi(uv[j]) * dsk);
    const u32 z = pk2(bflo(yb) + bflo(sk), bfhi(yb) + bfhi(sk));
    o[j] = pk2(bflo(xv[j]) + bflo(z), bfhi(xv[j]) + bfhi(z));
  }
  return o;
}

__global__ __launch_bounds__(512, 2) void k2_main(K2Args a) {
  extern __shared__ __attribute__((aligned(16))) char smem_[];
  LDS_AS char* smem = (LDS_AS char*)smem_;
  const int tid = threadIdx.x, lane = tid & 63;
  const int wid = __builtin_amdgcn_readfirstlane(tid >> 6);
  const int b = blockIdx.x, xcd = b & 7, rr = b >> 3;
  const int mt = (xcd & 1) * 4 + (rr & 3), ft = (xcd >> 1) * 8 + (rr >> 2);
  TS_DECL; TS(0);
  G1Ctx c;
  c.wid = wid;
  c.rot2k = (ft < 18 ? ft : ft - 18) * 2048;
  c.lds0 = (u32)(size_t)smem;
  c.ldsw = c.lds0 + wid * 1024;
  c.ldsa = c.ldsw + (wid >> 2) * 32;
  c.hTw = a.ws + WS_HT + mt * 512 + (size_t)(2 * wid) * (L * 2);
  c.Wt = a.ws + WS_WT + (size_t)ft * 9 * 36864;
  c.Wq = c.Wt + (1 + ((wid >> 1) & 1)) * 36864 + (wid & 1) * 1024;
  c.loff = lane * 16;
  const int arow = lane >> 5;
  c.aoff = arow * (L * 2) + (((lane & 31) ^ (((2 * wid + arow) & 3) << 2)) * 16);
  const int unit = b < 128 ? ((((b >> 4) * 9 + 2 * ((b >> 2) & 3) + 1)) << 2) + (b & 3) : 288 + (b - 128);
  u32x4 rv[5], rf[9];
  RtDst rd;
  {
    const int half = wid >> 2, wl = wid & 3, tt = tid & 255;
    const int dm = b + 256 * half;
    const int dc = 512 + (b >> 2), nlo = b & 3, nhi = 7 - nlo;
    const u32x4* krm = (const u32x4*)(a.ws + WS_KREV + (size_t)dm * KREV_PITCH);
    const u32x4* krc = (const u32x4*)(a.ws + WS_KREV + (size_t)dc * KREV_PITCH);
    const char* ux = a.ws + WS_UX + (size_t)(tt >> 2) * D * 128 + (tt & 3) * 16;
    const u32x4 k0 = krm[tt], k1 = krm[tt + 1], uv = *(const u32x4*)(ux + dm * 128);
    const u32x4 kc0 = krc[tt], kc1 = krc[tt + 1], uc = *(const u32x4*)(ux + dc * 128);
    const u32x4 xv = *(const u32x4*)(ux + dm * 128 + 64);
    const int te = ((tt & 32) ? nlo : nhi) * 32 + (tt & 31);
    const char* uxe = a.ws + WS_UX + ((size_t)(te >> 2) * D + dc) * 128 + (te & 3) * 16;
    const u32x4 ue = *(const u32x4*)uxe, xe = *(const u32x4*)(uxe + 64);
    const float dskm = bf2f(a.dskip[dm]), dskc = bf2f(a.dskip[dc]);
    const u32x4* rp[5];
    retile_addr<512, 5>(a.w1, a.w3, a.w2, unit, tid, rp);
#pragma unroll
    for (int i = 0; i < 5; i++) OPAQUE(rp[i]);
    LDS_AS char* cm = smem + CV_K0 + half * CV_BYTES;
    LDS_AS char* cc = smem + CV_K0 + 2 * CV_BYTES;
    cv_copies<0, 4>(cm, tt, k0, k1);
    *(LDS_AS u32x4*)(cm + CV_UOFF + 512 + tt * 16) = uv;
    if (half == 0) cv_copies<0, 2>(cc, tt, kc0, kc1);
    else { cv_copies<2, 2>(cc, tt, kc0, kc1); *(LDS_AS u32x4*)(cc + CV_UOFF + 512 + tt * 16) = uc; }
    {
      const u32x4 z = {0, 0, 0, 0};
      if (tid == 511) *(LDS_AS u32*)(smem + G1_CSOFF) = 0u;
      if (tid < 144) {
        const int ch = tid / 48, i = tid - ch * 48;
        *(LDS_AS u32x4*)(smem + CV_K0 + ch * CV_BYTES + (i < 16 ? (i >> 2) * CV_CP + 4096 + (i & 3) * 16 : CV_UOFF + (i - 16) * 16)) = z;
      }
    }
    __builtin_amdgcn_s_waitcnt(0x0070);
    BAR();
    TS(1);
    const int i16 = lane & 15, g = lane >> 4;
    const int aoff = ((3 - i16) & 3) * CV_CP + (2028 - (i16 & 12) + 8 * g) * 2;
    const int boff = CV_UOFF + (2 * i16 + g + 30) * 16;
    f32x4 acc[8], eh = {0, 0, 0, 0}, el = eh;
#pragma unroll
    for (int n = 0; n < 8; n++) acc[n] = (f32x4){0, 0, 0, 0};
    {
      LDS_AS char* ap = cm + aoff - 128 * wl;
      LDS_AS char* bp = cm + boff - 128 * wl;
      LDS_AS char* apc = cc + aoff - 64 * wid;
      LDS_AS char* bh = cc + boff - 64 * wid + 512 * nhi;
      LDS_AS char* bl = cc + boff - 64 * wid + 512 * nlo;
      u32x4 A[2][8], B[2][8], Ac[8], Bh[8], Bl[4];
      A[0][0] = cv_lda(ap);
#pragma unroll
      for (int j = 0; j < 8; j++) B[0][j] = *(LDS_AS u32x4*)(bp + 512 * j);
      A[0][1] = cv_lda(ap - 512);
      PIN();
#pragma unroll
      for (int a1 = 0; a1 < 8; a1++) {
        if (a1 + 2 < 8) A[0][a1 + 2] = cv_lda(ap - 512 * (a1 + 2));
        B[1][a1] = *(LDS_AS u32x4*)(bp - 64 + 512 * a1);
        if (a1 >= 6) A[1][a1 - 6] = cv_lda(ap - 64 - 512 * (a1 - 6));
        PIN();
#pragma unroll
        for (int n = a1; n < 8; n++) acc[n] = mfma16(A[0][a1], B[0][n - a1], acc[n]);
        if (a1 < 5) rv[a1] = __builtin_nontemporal_load(rp[a1]);
        PIN();
      }
#pragma unroll
      for (int a1 = 0; a1 < 8; a1++) {
        if (a1 + 2 < 8) A[1][a1 + 2] = cv_lda(ap - 64 - 512 * (a1 + 2));
        Ac[a1] = cv_lda(apc - 512 * a1);
        Bh[a1] = *(LDS_AS u32x4*)(bh - 512 * (a1 <= nhi ? a1 : nhi));
        if (a1 < 4) Bl[a1] = *(LDS_AS u32x4*)(bl - 512 * (a1 <= nlo ? a1 : nlo));
        PIN();
#pragma unroll
        for (int n = a1; n < 8; n++) acc[n] = mfma16(A[1][a1], B[1][n - a1], acc[n]);
        PIN();
      }
#pragma unroll
      for (int a1 = 0; a1 < 8; a1++) {
        if (a1 <= nhi) eh = mfma16(Ac[a1], Bh[a1], eh);
        if (a1 < 4 && a1 <= nlo) el = mfma16(Ac[a1], Bl[a1], el);
      }
    }
    BAR();
    LDS_AS char* pm = smem + CV_K0 + half * 32768;
    LDS_AS char* pc = smem + CV_K0 + 65536;
#pragma unroll
    for (int n = 0; n < 8; n++) *(LDS_AS f32x4*)(pm + wl * 8192 + n * 1024 + lane * 16) = acc[n];
    *(LDS_AS f32x4*)(pc + wid * 2048 + lane * 16) = eh;
    *(LDS_AS f32x4*)(pc + wid * 2048 + 1024 + lane * 16) = el;
    WAITL(0); BAR();
    {
      const int soff = ((tt >> 5) * 64 + ((tt & 31) >> 1) + 32 * (tt & 1)) * 16;
      f32x4 y0, y1;
      cv_sum<4, 8192>(pm + soff, y0, y1);
      st16_wt(mkrsrc(a.ws + WS_HT + (size_t)dm * (L * 2), L * 2), tt * 16, cv_out(y0, y1, uv, xv, dskm));
      if (wid == 4) {
        cv_sum<8, 2048>(pc + soff, y0, y1);
        st16_wt(mkrsrc(a.ws + WS_HT + (size_t)dc * (L * 2), L * 2), te * 16, cv_out(y0, y1, ue, xe, dskc));
      }
    }
    retile_stage<512, 5>(smem + G1_RT, unit, tid, rv);
    WAITV(0);
    __builtin_amdgcn_s_waitcnt(0x0070);
    BAR();
    TS(2);
    if (tid == 0) flags_add(a.ws + WS_FLAG + FL_A, b);
    rd = retile_read(a.ws, smem + G1_RT, unit, lane, wl, wid < 4, rf);
    WAITL(0);
    if (wid >= 4) {
      volatile LDS_AS u32* go = (volatile LDS_AS u32*)(smem + G1_CSOFF);
      if (wid == 4) { flags_spin(a.ws + WS_FLAG + FL_A, (lane & 15) * 128); *go = 1u; }
      else {
#pragma unroll 1
        for (int i = wid - 5; i < 36; i += 3) g1_frag<0>(c, i);
        for (u32 spins = 0; *go == 0u && spins < (1u << 24); ++spins) __builtin_amdgcn_s_sleep(1);
      }
      TS(3);
#pragma unroll 1
      for (int S = 0; S < 4; S++) {
        const u32 po = g1_rot(c, S * 2048), dst = c.ldsw - 4096 + G1_RING + S * G1_STAGE;
#pragma unroll
        for (int i = 0; i < 4; i++) dma16(c.hTw + (i - 1) * 8 * (L * 2), c.aoff + po * 64u, dst + i * 4128);
        dma16(c.Wq, c.loff + po, dst + G1_ACT);
      }
      WAITV(15);
    }
  }
  const int hh = lane >> 5, g4 = lane >> 4, q4 = (lane & 15) >> 2, p4 = lane & 3;
#pragma unroll
  for (int i = 0; i < 3; i++) {
    c.ab.b[i] = smem + (8 * g4 + q4) * 512 + g4 * 32 + ((wid ^ q4) * 64) + p4 * 8 + i * 49152;
    c.wb.b[i] = smem + lane * 16 + i * 49152;
    OPAQUE(c.ab.b[i]); OPAQUE(c.wb.b[i]);
  }
  G1Stat st;
  st.stat = a.ws + WS_STAT + (size_t)mt * 18 * 2048;
  st.flag = a.ws + WS_FLAG;
  st.csp = a.ws + WS_CSP + ft * 8192;
  st.soff = hh * 3 * 2048 + (wid * 32 + (lane & 31)) * 8;
  st.coff = tid * 16;
  st.foff = (lane & 15) * 128;
  st.ft = ft; st.b = b;
  u32x4 act[36], w[3];
  G1Acc acc[4] = {};
  BAR();
  TS(4);
  g1_stage3<4>(c); g1_stage3<5>(c);
  act[0] = g1_lda<0>(c); act[1] = g1_lda<1>(c);
  w[0] = g1_ldw1<0, 0>(c); w[1] = g1_ldw1<0, 1>(c); w[2] = g1_ldw1<0, 2>(c);
  PIN();
  st.rd = rd;
  G1P1Step<0>::run(c, acc, w, act, st, rf, lane);
  TS(5);
  asm volatile("" : "+v"(st.g[0]), "+v"(st.g[1]), "+v"(st.g[2]), "+v"(st.cs));
  *(LDS_AS float*)(smem + G1_CSOFF + tid * 4) = (__uint_as_float(st.cs[0]) + __uint_as_float(st.cs[1])) + (__uint_as_float(st.cs[2]) + __uint_as_float(st.cs[3]));
  {
    bool bad = st.g[0].y == ~0u || st.g[1].y == ~0u || st.g[2].y == ~0u;
    for (u32 spins = 0; __any(bad) && spins < (1u << 22); ++spins) {
      __builtin_amdgcn_s_sleep(2);
      bad = false;
#pragma unroll
      for (int i = 0; i < 3; i++) {
        asm volatile("global_load_dwordx2 %0, %1, %2 sc1\n\ts_waitcnt vmcnt(0)" : "=&v"(st.g[i]) : "v"(st.soff), "s"(st.stat + i * 2048) : "memory");
        bad |= (st.g[i].y == ~0u);
      }
    }
  }
  float s1 = 0.f, s2 = 0.f;
#pragma unroll
  for (int i = 0; i < 3; i++) { s1 += __uint_as_float(st.g[i].x); s2 += __uint_as_float(st.g[i].y); }
  s1 += __shfl_xor(s1, 32); s2 += __shfl_xor(s2, 32);
  const float mean = s1 * (1.0f / D);
  const float var = fmaxf(s2 * (1.0f / D) - mean * mean, 0.f);
  const float mu = rbf(mean);
  const float sd = rbf(sqrtf(rbf(rbf(var) + 1e-5f)));
  const float mu2 = __shfl_xor(mu, 16), sdo = __shfl_xor(sd, 16);
  G1Row row;
  row.mu[0] = (lane & 16) ? mu2 : mu; row.mu[1] = (lane & 16) ? mu : mu2;
  const float sdA = (lane & 16) ? sdo : sd, sdB = (lane & 16) ? sd : sdo;
  row.sd2[0] = sdA * sdA; row.sd2[1] = sdB * sdB;
  row.nrl2[0] = -1.44269504088896341f / sdA; row.nrl2[1] = -1.44269504088896341f / sdB;
  auto rsh = mkrsrc(a.ws + WS_HFF, (u32)HFF_BYTES);
  const u32 voff = (u32)(mt * 16 + wid * 2 + (g4 & 1)) * (144 * 1024) + (u32)((lane & 15) + 16 * hh) * 16;

  const int ks = xcd >> 1, mt16 = (xcd & 1) * 8 + (rr & 7), nt = rr >> 3;
  const int tile = mt16 * 4 + nt;
  c.asrc = a.ws + WS_HFF + ((size_t)(mt16 * 8 + 2 * (wid & 3)) * 144 + ks * 36) * 1024;
  c.wsrc = a.ws + WS_W2T + ((size_t)nt * 144 + ks * 36) * 9216 + (wid & 3) * 1024;
  u32* fl_out = (u32*)(a.ws + WS_FLAG + FL_HFF + (size_t)(((mt * 2 + (wid >> 2)) * 4 + (ft >> 3)) * 3) * 128);
  const char* fl_in = a.ws + WS_FLAG + FL_HFF + (size_t)((mt16 * 4 + ks) * 3) * 128;
  auto publish = [&](int f) { if (lane == 0) __hip_atomic_fetch_add(fl_out + f * 32, 1u, __ATOMIC_RELAXED, __HIP_MEMORY_SCOPE_AGENT); };
  u32 pollv;
  auto poll_issue = [&](int f) { asm volatile("global_load_dword %0, %1, %2 sc1" : "=v"(pollv) : "v"(0u), "s"(fl_in + f * 128) : "memory"); };
  auto poll_check = [&](int f) {
    asm volatile("" : "+v"(pollv));
    for (u32 spins = 0; __builtin_amdgcn_readfirstlane(pollv) < 32u && spins < (1u << 22); ++spins) {
      __builtin_amdgcn_s_sleep(2);
      asm volatile("global_load_dword %0, %1, %2 sc1\n\ts_waitcnt vmcnt(0)" : "=&v"(pollv) : "v"(0u), "s"(fl_in + f * 128) : "memory");
    }
  };

#define EPI(P, A) g1_epilogue<P>(A, smem, g4, row, rsh, voff, ft); PIN()
  const bool early = wid >= 4;
  WAITL(0); BAR();
  asm volatile("global_load_dwordx4 %0, %1, %2 sc1" : "=v"(st.cs) : "v"(st.coff), "s"(st.csp) : "memory");
  if (early) { EPI(0, acc[0]); EPI(1, acc[1]); }
  acc[3] = g1_pass<3>(act, c);
  if (!early) { EPI(0, acc[0]); EPI(1, acc[1]); }
  WAITV(9);
  asm volatile("" : "+v"(st.cs));
  if (tid >= 160) *(LDS_AS float*)(smem + G1_CSOFF + tid * 4) = (__uint_as_float(st.cs[0]) + __uint_as_float(st.cs[1])) + (__uint_as_float(st.cs[2]) + __uint_as_float(st.cs[3]));
  WAITL(0); BAR();
  if (early) { EPI(2, acc[2]); EPI(3, acc[3]); }
  acc[0] = g1_pass<4>(act, c);
  if (!early) { EPI(2, acc[2]); EPI(3, acc[3]); }
  TS(6);
  WAITV(7);
  publish(0);
  BAR();
  if (early) { EPI(4, acc[0]); }
  acc[1] = g1_pass<5>(act, c);
  if (!early) { poll_issue(0); EPI(4, acc[0]); EPI(5, acc[1]); WAITV(2); poll_check(0); }
  else WAITV(7);
  BAR();
  if (early) { EPI(5, acc[1]); }
  acc[2] = g1_pass<6>(act, c);
  if (!early) { EPI(6, acc[2]); } else WAITV(1);
  BAR();
  if (early) { EPI(6, acc[2]); }
  acc[3] = g1_pass<7>(act, c);
  if (!early) { EPI(7, acc[3]); WAITV(9); } else WAITV(1);
  publish(1);
  TS(7);
  BAR();
  if (early) { EPI(7, acc[3]); }
  acc[0] = g1_pass<8>(act, c);
  EPI(8, acc[0]);
  TS(8);
#undef EPI
  BAR();

  u32* cnt_in = (u32*)(a.ws + WS_FLAG + (size_t)(tile * 4 + ks) * 128);
  if (!early) {
    poll_issue(1);
    g2_stage_dma(c, 3, g2_slot(3));
    {
      const char* hsrc = a.ws + WS_HT + (size_t)(nt * 144) * (L * 2) + (mt16 * 128 + ks * 32) * 2;
      const u32 hoff = (lane >> 2) * (L * 2) + (lane & 3) * 16;
#pragma unroll
      for (int i = 0; i < 3; i++) { const int pc = (wid + 4 * i < 9) ? wid + 4 * i : wid; dma16(hsrc + (size_t)pc * 16 * (L * 2), hoff, c.lds0 + G2_RES + pc * 1024); }
    }
    WAITV(11);
    poll_check(1);
    publish(2);
    BAR();
    g2_stage_dma(c, 4, g2_slot(0));
    BAR();
    g2_stage_dma(c, 5, g2_slot(1));
#define G2_LD(s) { if (wid < 2) WAITV(18); else WAITV(16); BAR(); g2_stage_dma(c, (s) + 4, g2_slot(s)); }
    G2_LD(2) G2_LD(3) G2_LD(4)
    poll_issue(2);
    G2_LD(5) G2_LD(6) G2_LD(7)
    poll_check(2);
    G2_LD(8) G2_LD(9) G2_LD(10) G2_LD(11) G2_LD(12) G2_LD(13)
#undef G2_LD
    if (wid < 2) WAITV(18); else WAITV(16);
    BAR();
    WAITV(8); BAR();
    WAITV(0); BAR();
  } else {
    const int mw = wid - 4;
    G1Bases ab;
#pragma unroll
    for (int i = 0; i < 3; i++) { ab.b[i] = smem + mw * 4096 + lane * 16 + i * 49152; OPAQUE(ab.b[i]); }
    f32x4 o[2][9];
#pragma unroll
    for (int i = 0; i < 2; i++)
#pragma unroll
      for (int j = 0; j < 9; j++) { o[i][j] = (f32x4){0, 0, 0, 0}; OPAQUE(o[i][j]); }
    u32x4 A0[2], W0[9], A1[2], W1[9];
    constexpr int S0 = g2_slot(0);
    A0[0] = g1_ldw<S0>(ab); A0[1] = g1_ldw<S0 + 2048>(ab);
    W0[0] = g1_ldw<S0 + 16384>(c.wb); W0[1] = g1_ldw<S0 + 17408>(c.wb); W0[2] = g1_ldw<S0 + 18432>(c.wb); W0[3] = g1_ldw<S0 + 19456>(c.wb);
    W0[4] = g1_ldw<S0 + 20480>(c.wb); W0[5] = g1_ldw<S0 + 21504>(c.wb); W0[6] = g1_ldw<S0 + 22528>(c.wb); W0[7] = g1_ldw<S0 + 23552>(c.wb);
    W0[8] = g1_ldw<S0 + 24576>(c.wb);
    PIN();
#pragma unroll 1
    for (int it = 0; it < 4; it++) {
      g2_stage<0, false>(o, A0, W0, A1, W1, ab, c.wb);
      g2_stage<1, false>(o, A0, W0, A1, W1, ab, c.wb);
      if (it == 0) { WAITV(0); publish(2); }
      g2_stage<2, false>(o, A0, W0, A1, W1, ab, c.wb);
      g2_stage<3, false>(o, A0, W0, A1, W1, ab, c.wb);
    }
    g2_stage<0, false>(o, A0, W0, A1, W1, ab, c.wb);
    g2_stage<1, true>(o, A0, W0, A1, W1, ab, c.wb);
    TS(9);
    if (mw != ks) {
      const auto rs_out = mkrsrc(a.ws + WS_PART + ((size_t)(tile * 4 + mw) * 4 + ks) * 9216, 9216);
#pragma unroll
      for (int j = 0; j < 9; j++) {
        u32x4 v = {pk2(o[0][j][0], o[0][j][1]), pk2(o[0][j][2], o[0][j][3]), pk2(o[1][j][0], o[1][j][1]), pk2(o[1][j][2], o[1][j][3])};
        st16_wt(rs_out, (j * 64 + lane) * 16, v);
      }
      WAITV(0);
      if (lane == 0) __hip_atomic_fetch_add((u32*)(a.ws + WS_FLAG + (size_t)(tile * 4 + mw) * 128), 1u, __ATOMIC_RELAXED, __HIP_MEMORY_SCOPE_AGENT);
    } else {
#pragma unroll
      for (int j = 0; j < 9; j++) {
        *(LDS_AS f32x4*)(smem + g2_slot(2) + ((j * 2) * 64 + lane) * 16) = o[0][j];
        *(LDS_AS f32x4*)(smem + g2_slot(2) + ((j * 2 + 1) * 64 + lane) * 16) = o[1][j];
      }
      WAITL(0);
    }
  }
  BAR();
  {
    u32 spins = 0;
    while (__builtin_amdgcn_readfirstlane(__hip_atomic_load(cnt_in, __ATOMIC_RELAXED, __HIP_MEMORY_SCOPE_AGENT)) < 3u) {
      __builtin_amdgcn_s_sleep(1);
      if (++spins > (1u << 24)) break;
    }
    __builtin_amdgcn_fence(__ATOMIC_ACQUIRE, "wavefront");
    TS(10);
    const auto rs_in = mkrsrc(a.ws + WS_PART + (size_t)(tile * 4 + ks) * 4 * 9216, 4 * 9216);
    int ln = threadIdx.x; asm volatile("" : "+v"(ln)); ln &= 63;
    const int lane = ln, g = ln >> 4, q4 = (ln & 15) >> 2, p4 = ln & 3;
    for (int j = wid; j < 9; j += 8) {
      u32x4 pp[3];
#pragma unroll
      for (int sidx = 0; sidx < 3; sidx++)
        pp[sidx] = __builtin_amdgcn_raw_buffer_load_b128(rs_in, ((ks + 1 + sidx) & 3) * 9216 + (j * 64 + lane) * 16, 0, 16);
      LDS_AS char* rb = smem + G2_RES + (j * 16 + 4 * g + q4) * 64 + p4 * 8;
      u16* orow = a.out + (size_t)(mt16 * 128 + ks * 32 + (lane & 15)) * D + nt * 144 + j * 16 + 4 * g;
#pragma unroll
      for (int i = 0; i < 2; i++) {
        const u32x2 hres = __builtin_bit_cast(u32x2, tr_read(rb + i * 32));
        f32x4 v = *(LDS_AS f32x4*)(smem + g2_slot(2) + ((j * 2 + i) * 64 + lane) * 16);
#pragma unroll
        for (int sidx = 0; sidx < 3; sidx++) {
          v[0] += bflo(pp[sidx][2 * i]); v[1] += bfhi(pp[sidx][2 * i]);
          v[2] += bflo(pp[sidx][2 * i + 1]); v[3] += bfhi(pp[sidx][2 * i + 1]);
        }
        const u32 o01 = pk2(v[0], v[1]), o23 = pk2(v[2], v[3]);
        u32x2 ov = {pk2(bflo(hres.x) + bflo(o01), bfhi(hres.x) + bfhi(o01)), pk2(bflo(hres.y) + bflo(o23), bfhi(hres.y) + bfhi(o23))};
        *(u32x2*)(orow + (size_t)i * 16 * D) = ov;
      }
    }
  }
  TS(11);
  TS_OUT(1);
}

extern "C" void kernel_launch(void* const* d_in, const int* in_sizes, int n_in, void* d_out, int out_size, void* d_ws,
                              size_t ws_size, hipStream_t stream) {
  (void)in_sizes; (void)n_in; (void)out_size; (void)ws_size;
  (void)hipFuncSetAttribute((const void*)k2_main, hipFuncAttributeMaxDynamicSharedMemorySize, K2_LDS);
  K1Args a1;
  a1.x = (const u16*)d_in[0]; a1.Are = (const u16*)d_in[1]; a1.Aim = (const u16*)d_in[2];
  a1.Bre = (const u16*)d_in[3]; a1.Bim = (const u16*)d_in[4]; a1.Cre = (const u16*)d_in[5];
  a1.Cim = (const u16*)d_in[6]; a1.ldt = (const u16*)d_in[7]; a1.w1 = (const u16*)d_in[10];
  a1.w3 = (const u16*)d_in[11]; a1.w2 = (const u16*)d_in[12]; a1.ws = (char*)d_ws;
  k1_pre<<<K1_LN + K1_KG + K1_PT + (K1ORD == 2 ? 64 : 0), 256, K1_LDS, stream>>>(a1);
  K2Args a2;
  a2.dskip = (const u16*)d_in[8]; a2.w1 = (const u16*)d_in[10]; a2.w3 = (const u16*)d_in[11]; a2.w2 = (const u16*)d_in[12];
  a2.ws = (char*)d_ws; a2.out = (u16*)d_out;
  k2_main<<<256, 512, K2_LDS, stream>>>(a2);
}
```

```cpp
#include <hip/hip_runtime.h>
#include <stdint.h>

typedef unsigned short u16;
typedef unsigned int u32;
typedef unsigned long long u64;
typedef __bf16 bf2 __attribute__((ext_vector_type(2)));
typedef __bf16 bf8 __attribute__((ext_vector_type(8)));
typedef short s4 __attribute__((ext_vector_type(4)));
typedef u32 u32x2 __attribute__((ext_vector_type(2)));
typedef u32 u32x4 __attribute__((ext_vector_type(4)));
typedef float f32x2 __attribute__((ext_vector_type(2)));
typedef float f32x4 __attribute__((ext_vector_type(4)));
#define LDS_AS __attribute__((address_space(3)))
#define DEVI __device__ __forceinline__

constexpr int L = 2048, D = 576, NM = 128, F = 4608;

constexpr size_t WS_UX   = 0;
constexpr size_t WS_HT   = WS_UX + (size_t)D * L * 4;
constexpr int    KREV_PITCH = 4352;
constexpr size_t WS_KREV = WS_HT + (size_t)D * L * 2;
constexpr size_t WS_WT   = WS_KREV + (size_t)D * KREV_PITCH;
constexpr size_t WT_BYTES = (size_t)288 * 36 * 1024;
constexpr size_t WS_W2T  = WS_WT + WT_BYTES + 65536;
constexpr size_t W2T_BYTES = (size_t)4 * 144 * 9 * 1024;
constexpr size_t WS_CSP  = WS_W2T + W2T_BYTES + 65536;
constexpr size_t WS_HFF  = WS_CSP + (size_t)32 * 4 * 512 * 4;
constexpr size_t HFF_BYTES = (size_t)128 * 144 * 1024;
constexpr size_t WS_PART = WS_HFF + HFF_BYTES;
constexpr size_t PART_BYTES = (size_t)64 * 4 * 4 * 9216;
constexpr size_t WS_FLAG = WS_PART + PART_BYTES;
constexpr size_t FLAG_BYTES = (size_t)512 * 128;
constexpr size_t WS_STAT = WS_FLAG + FLAG_BYTES;
constexpr size_t STAT_BYTES = (size_t)8 * 18 * 256 * 8;

DEVI float bf2f(u32 v) { return __uint_as_float(v << 16); }
DEVI float bflo(u32 v) { return __uint_as_float(v << 16); }
DEVI float bfhi(u32 v) { return __uint_as_float(v & 0xffff0000u); }
DEVI u32 pk2(float lo, float hi) {
  bf2 r = {(__bf16)lo, (__bf16)hi};
  return __builtin_bit_cast(u32, r);
}
DEVI float rbf(float x) { return __uint_as_float(((u32)__builtin_bit_cast(u16, (__bf16)x)) << 16); }
DEVI void dma16(const char* sbase, u32 voff, u32 lds) {
  asm volatile("s_mov_b32 m0, %2\n\ts_nop 0\n\tglobal_load_lds_dwordx4 %0, %1 sc1" ::"v"(voff), "s"(sbase), "s"(lds) : "memory");
}
#define OPAQUE(x) asm volatile("" : "+v"(x))
#define PIN() __builtin_amdgcn_sched_barrier(0)
DEVI s4 tr_read(LDS_AS void* p) { return __builtin_amdgcn_ds_read_tr16_b64_v4i16((LDS_AS s4*)p); }
DEVI u32x4 tr_read2(LDS_AS char* p0, LDS_AS char* p1) {
  u32x2 a = __builtin_bit_cast(u32x2, tr_read(p0)), b = __builtin_bit_cast(u32x2, tr_read(p1));
  u32x4 r = {a.x, a.y, b.x, b.y};
  return r;
}
DEVI void st16_wt(__amdgpu_buffer_rsrc_t rs, u32 off, u32x4 v) { __builtin_amdgcn_raw_buffer_store_b128(v, rs, off, 0, 16); }
DEVI __amdgpu_buffer_rsrc_t mkrsrc(const void* p, u32 bytes) {
  return __builtin_amdgcn_make_buffer_rsrc((void*)p, 0, bytes, 0x00020000);
}
#define WAITV(n) asm volatile("s_waitcnt vmcnt(" #n ")" ::: "memory")
#define WAITL(n) asm volatile("s_waitcnt lgkmcnt(" #n ")" ::: "memory")
#define BAR() do { asm volatile("" ::: "memory"); __builtin_amdgcn_s_barrier(); asm volatile("" ::: "memory"); } while (0)
DEVI f32x4 mfma16(u32x4 a, u32x4 b, f32x4 c) {
  return __builtin_amdgcn_mfma_f32_16x16x32_bf16(__builtin_bit_cast(bf8, a), __builtin_bit_cast(bf8, b), c, 0, 0, 0);
}
typedef _Float16 h2v __attribute__((ext_vector_type(2)));
typedef _Float16 h8v __attribute__((ext_vector_type(8)));
DEVI u32 pkh2(float lo, float hi) { h2v r = {(_Float16)lo, (_Float16)hi}; return __builtin_bit_cast(u32, r); }
DEVI f32x4 mfma16h(u32x4 a, u32x4 b, f32x4 c) {
  return __builtin_amdgcn_mfma_f32_16x16x32_f16(__builtin_bit_cast(h8v, a), __builtin_bit_cast(h8v, b), c, 0, 0, 0);
}
template <int CTRL> DEVI float dpp(float v) {
  return __builtin_bit_cast(float, __builtin_amdgcn_update_dpp(0, __builtin_bit_cast(int, v), CTRL, 0xf, 0xf, true));
}
DEVI float sum8(float v) {
  v += dpp<0xB1>(v); v += dpp<0x4E>(v); v += dpp<0x141>(v);
  return v;
}
DEVI float dot2(u32 a, u32 b, float c) {
  return __builtin_amdgcn_fdot2_f32_bf16(__builtin_bit_cast(bf2, a), __builtin_bit_cast(bf2, b), c, false);
}


constexpr int K1_KG = 576, K1_LN = 128, K1_PT = 176;
constexpr int K1_LDS = 38 * 1024;

struct K1Args {
  const u16 *x, *Are, *Aim, *Bre, *Bim, *Cre, *Cim, *ldt, *w1, *w3, *w2;
  char* ws;
};

constexpr int lag_index(int l) {
  if (l < 256) return l;
  int e = 0;
  while ((l >> e) >= 256) e++;
  int q = l >> e;
  const int rem = l & ((1 << e) - 1), half = 1 << (e - 1);
  if (rem > half || (rem == half && (q & 1))) q++;
  return 128 * e + q;
}
constexpr int kg_p1(int r) { return r < 8 ? 256 + 2 * r : 512 + 256 * ((r - 8) >> 2) + 4 * ((r - 8) & 3); }
constexpr int kg_p2(int r) { return r < 8 ? 1024 + 256 * (r >> 1) + 8 * (r & 1) : 2048; }
struct KgTab {
  u16 g[256][8]; u16 t[256][2];
  constexpr KgTab() : g(), t() {
    for (int i = 0; i < 256; i++) {
      for (int j = 0; j < 8; j++) g[i][j] = (u16)(4 * lag_index(2047 - (i * 8 + j)));
      const int r = i & 15, c16 = (i >> 4) * 16;
      t[i][0] = (u16)(4 * lag_index(kg_p1(r) + c16));
      t[i][1] = (u16)(4 * ((r < 8 || i == 8) ? lag_index(kg_p2(r) + c16) : 700));
    }
  }
};
__constant__ __attribute__((aligned(16))) const KgTab kg_tab{};

DEVI int panel_log(int ft, int p) { const int ls = 4 - (ft & 3); return p < ls ? p : (p < ls + 4 ? p - ls + 5 : p - 4); }
DEVI int panel_phys(int ft, int lp) { const int ls = 4 - (ft & 3); return lp < ls ? lp : (lp < 5 ? lp + 4 : lp - 5 + ls); }

template <int NT, int NV>
DEVI void retile_addr(const u16* w1, const u16* w3, const u16* w2, int unit, int tid, const u32x4* (&p)[NV]) {
  if (unit < 288) {
    const int fq = unit >> 2, kq = unit & 3, ch = tid & 15;
    const u16* src = ((ch >> 3) ? w3 : w1) + (size_t)(kq * 144) * F + fq * 64 + (ch & 7) * 8;
#pragma unroll
    for (int i = 0; i < NV; i++) {
      int k = (tid + NT * i) >> 4; k = k > 143 ? 143 : k;
      p[i] = (const u32x4*)(src + (size_t)k * F);
    }
  } else {
    const int sg = unit - 288, ks = sg / 36, q = sg - ks * 36;
    const int fa = ks * 8 + (q < 32 ? (q & 7) : 2 * (q - 32)), fb = q < 32 ? fa : fa + 1;
    const int Pa = fa * 9 + panel_phys(fa, q < 32 ? 2 * (q >> 3) : 8), Pb = fb * 9 + panel_phys(fb, q < 32 ? 2 * (q >> 3) + 1 : 8);
    const u32x4* srcA = (const u32x4*)(w2 + (size_t)Pa * 16 * D);
    const u32x4* srcB = (const u32x4*)(w2 + (size_t)Pb * 16 * D) - 1152;
#pragma unroll
    for (int i = 0; i < NV; i++) {
      int c = tid + NT * i; c = c > 2303 ? 2303 : c;
      p[i] = (c < 1152 ? srcA : srcB) + c;
    }
  }
}
template <int NT, int NV>
DEVI void retile_load(const u16* w1, const u16* w3, const u16* w2, int unit, int tid, u32x4 (&v)[NV]) {
  const u32x4* p[NV];
  retile_addr<NT, NV>(w1, w3, w2, unit, tid, p);
#pragma unroll
  for (int i = 0; i < NV; i++) v[i] = __builtin_nontemporal_load(p[i]);
}
template <int NT, int NV>
DEVI void retile_stage(LDS_AS char* smem, int unit, int tid, const u32x4 (&v)[NV]) {
  if (unit < 288) {
    const int ch = tid & 15;
#pragma unroll
    for (int i = 0; i < NV; i++) {
      int k = (tid + NT * i) >> 4; k = k > 143 ? 143 : k;
      *(LDS_AS u32x4*)(smem + k * 256 + (((ch >> 1) ^ (k & 3)) * 32) + (ch & 1) * 16) = v[i];
    }
  } else {
#pragma unroll
    for (int i = 0; i < NV; i++) {
      int c = tid + NT * i; c = c > 2303 ? 2303 : c;
      const int r = c / 72, cc = c - r * 72;
      *(LDS_AS u32x4*)(smem + r * 1152 + ((cc ^ (r & 6)) * 16)) = v[i];
    }
  }
}
struct RtDst { const char* base; u32 voff; int k0; };
DEVI u32 rt_off(const RtDst& d, int i) { return d.k0 < 0 ? i * 1024 : ((d.k0 + i) >> 1) * 2048 + ((d.k0 + i) & 1) * 512; }
DEVI RtDst retile_read(char* ws, LDS_AS char* smem, int unit, int lane, int wid, bool pub, u32x4 (&fr)[9]) {
  const int q4 = (lane & 15) >> 2, p4 = lane & 3;
  RtDst d;
  if (unit < 288) {
    const int fq = unit >> 2, kq = unit & 3;
    const int P = fq * 4 + wid, ft = P / 9, lp = panel_log(ft, P - ft * 9);
    const int mat = lane >> 5, g2 = (lane >> 4) & 1;
    float cs = 0.f;
#pragma unroll
    for (int kk = 0; kk < 9; kk++) {
      LDS_AS char* p0 = smem + (kk * 16 + 8 * g2 + q4) * 256 + (((mat * 4 + wid) ^ q4) * 32) + p4 * 8;
      fr[kk] = tr_read2(p0, p0 + 4 * 256);
#pragma unroll
      for (int j = 0; j < 4; j++) cs = dot2(fr[kk][j], 0x3f803f80u, cs);
    }
    cs += __shfl_xor(cs, 16);
    if (pub && g2 == 0) {
      float* csp = (float*)(ws + WS_CSP) + ((size_t)ft * 512 + lp * 32 + mat * 16 + (lane & 15)) * 4 + kq;
      __hip_atomic_store(csp, cs, __ATOMIC_RELAXED, __HIP_MEMORY_SCOPE_AGENT);
    }
    d.base = ws + WS_WT + (size_t)(ft * 9 + lp) * 36864; d.voff = mat * 1024 + (lane & 31) * 16; d.k0 = kq * 9;
  } else {
    const int sg = unit - 288;
    const int row0 = 8 * (lane >> 4) + q4;
#pragma unroll
    for (int nf = 0; nf < 9; nf++) {
      LDS_AS char* p0 = smem + row0 * 1152 + (((wid * 9 + nf) ^ ((row0 >> 1) & 3)) * 32) + p4 * 8;
      LDS_AS char* p1 = smem + (row0 + 4) * 1152 + (((wid * 9 + nf) ^ (((row0 + 4) >> 1) & 3)) * 32) + p4 * 8;
      fr[nf] = tr_read2(p0, p1);
    }
    d.base = ws + WS_W2T + (size_t)(wid * 144 + sg) * 9216; d.voff = lane * 16; d.k0 = -1;
  }
  return d;
}

__global__ __launch_bounds__(256, 4) void k1_pre(K1Args a) {
  extern __shared__ __attribute__((aligned(16))) char smem_[];
  LDS_AS char* smem = (LDS_AS char*)smem_;
  const int tid = threadIdx.x, lane = tid & 63;
  const int wid = __builtin_amdgcn_readfirstlane(tid >> 6);
  const int b = blockIdx.x;
  if (b < K1_KG) {
    const int d = b;
    LDS_AS f32x4* prm = (LDS_AS f32x4*)(smem + 16384);
    if (tid < NM) {
      const int n = d * NM + tid;
#define LDP(p) bf2f(__builtin_nontemporal_load(p + n))
      const float Are = LDP(a.Are), Aim = LDP(a.Aim), Bre = LDP(a.Bre), Bim = LDP(a.Bim);
      const float cr = LDP(a.Cre), ci = LDP(a.Cim);
      const float dt = rbf(expf(bf2f(a.ldt[d])));
      const float br = dt * Bre, bi = dt * Bim;
      f32x4 v = {dt * Are * 1.44269504088896341f,
                 dt * Aim * 0.15915494309189535f,
                 cr * br - ci * bi, cr * bi + ci * br};
      prm[tid] = v;
    }
    const int r = lane & 15, kq = lane >> 4;
    const float p0 = (float)r, p1 = (float)kg_p1(r), p2 = (float)kg_p2(r), pY = (float)(16 * r);
    const u32x4 gi = *(const u32x4*)kg_tab.g[tid];
    const u32 ti = *(const u32*)kg_tab.t[tid];
    WAITL(0); BAR();
    f32x4 acc0 = {0, 0, 0, 0}, acc1 = acc0, acc2 = acc0;
    const f32x4 qm = prm[32 * wid + (lane & 31)];
    float cm = fabsf(qm[2]) + fabsf(qm[3]);
    cm = fmaxf(cm, dpp<0xB1>(cm)); cm = fmaxf(cm, dpp<0x4E>(cm)); cm = fmaxf(cm, dpp<0x141>(cm)); cm = fmaxf(cm, dpp<0x140>(cm));
    const float cmax = fmaxf(__builtin_bit_cast(float, __builtin_amdgcn_readlane(__builtin_bit_cast(int, cm), 0)),
                             __builtin_bit_cast(float, __builtin_amdgcn_readlane(__builtin_bit_cast(int, cm), 16)));
    const int E = __builtin_amdgcn_frexp_expf(cmax);
    if (__all(qm[0] <= 0.f && fabsf(qm[1]) <= 0.125f && E > -90 && E < 90)) {
      const float ey = (float)(14 - E);
      const float oscale = __builtin_amdgcn_ldexpf(1.0f, E - 28);
#pragma unroll
      for (int s = 0; s < 2; s++) {
        u32x4 x0, x1, x2, y;
#pragma unroll
        for (int m4 = 0; m4 < 4; m4++) {
          const f32x4 q = prm[32 * wid + 16 * s + 4 * kq + m4];
          const f32x2 q01 = {q[0], q[1]};
          auto zpow = [&](float p, float sc) {
            const f32x2 pp = {p, p}, ss = {sc, 0.f};
            const f32x2 ar = __builtin_elementwise_fma(q01, pp, ss);
            const float e = __builtin_amdgcn_exp2f(ar[0]);
            const f32x2 cs = {__builtin_amdgcn_cosf(ar[1]), __builtin_amdgcn_sinf(ar[1])}, ee = {e, e};
            return cs * ee;
          };
          const f32x2 yv = zpow(pY, ey);
          y[m4] = pkh2(q[2] * yv[0] - q[3] * yv[1], -(q[2] * yv[1] + q[3] * yv[0]));
          f32x2 v;
          v = zpow(p0, 14.f); x0[m4] = pkh2(v[0], v[1]);
          v = zpow(p1, 14.f); x1[m4] = pkh2(v[0], v[1]);
          v = zpow(p2, 14.f); x2[m4] = pkh2(v[0], v[1]);
        }
        acc0 = mfma16h(x0, y, acc0); acc1 = mfma16h(x1, y, acc1); acc2 = mfma16h(x2, y, acc2);
      }
      acc0 *= oscale; acc1 *= oscale; acc2 *= oscale;
    } else {
#pragma unroll 1
      for (int s = 0; s < 2; s++) {
        u32x4 xh0, xl0, xh1, xl1, xh2, xl2, yh, yl;
#pragma unroll
        for (int m4 = 0; m4 < 4; m4++) {
          const f32x4 q = prm[32 * wid + 16 * s + 4 * kq + m4];
          auto zpow = [&](float p, float& re, float& im) {
            float e = __builtin_amdgcn_exp2f(q[0] * p);
            float ph = __builtin_amdgcn_fractf(q[1] * p);
            re = e * __builtin_amdgcn_cosf(ph); im = e * __builtin_amdgcn_sinf(ph);
          };
          auto split = [&](float re, float im, u32& hi, u32& lo) {
            hi = pk2(re, im);
            lo = pk2(re - bflo(hi), im - bfhi(hi));
          };
          float yr, yi, re, im; u32 h, l;
          zpow(pY, yr, yi);
          split(q[2] * yr - q[3] * yi, -(q[2] * yi + q[3] * yr), h, l); yh[m4] = h; yl[m4] = l;
          zpow(p0, re, im); split(re, im, h, l); xh0[m4] = h; xl0[m4] = l;
          zpow(p1, re, im); split(re, im, h, l); xh1[m4] = h; xl1[m4] = l;
          zpow(p2, re, im); split(re, im, h, l); xh2[m4] = h; xl2[m4] = l;
        }
        acc0 = mfma16(xl0, yh, acc0); acc1 = mfma16(xl1, yh, acc1); acc2 = mfma16(xl2, yh, acc2);
        acc0 = mfma16(xh0, yl, acc0); acc1 = mfma16(xh1, yl, acc1); acc2 = mfma16(xh2, yl, acc2);
        acc0 = mfma16(xh0, yh, acc0); acc1 = mfma16(xh1, yh, acc1); acc2 = mfma16(xh2, yh, acc2);
      }
    }
    LDS_AS float* part = (LDS_AS float*)smem;
    *(LDS_AS f32x4*)(part + ((wid * 3 + 0) * 16 + r) * 16 + 4 * kq) = acc0;
    *(LDS_AS f32x4*)(part + ((wid * 3 + 1) * 16 + r) * 16 + 4 * kq) = acc1;
    *(LDS_AS f32x4*)(part + ((wid * 3 + 2) * 16 + r) * 16 + 4 * kq) = acc2;
    __syncthreads();
    LDS_AS float* tab = part + 4 * 768;
    LDS_AS char* tb = (LDS_AS char*)tab;
    tab[tid] = (part[tid] + part[768 + tid]) + (part[2 * 768 + tid] + part[3 * 768 + tid]);
    *(LDS_AS float*)(tb + (ti & 0xffffu)) = (part[256 + tid] + part[768 + 256 + tid]) + (part[2 * 768 + 256 + tid] + part[3 * 768 + 256 + tid]);
    *(LDS_AS float*)(tb + (ti >> 16)) = (part[512 + tid] + part[768 + 512 + tid]) + (part[2 * 768 + 512 + tid] + part[3 * 768 + 512 + tid]);
    __syncthreads();
    auto rsk = mkrsrc(a.ws + WS_KREV + (size_t)d * KREV_PITCH, KREV_PITCH);
    u32x4 o;
#pragma unroll
    for (int j = 0; j < 4; j++) o[j] = pk2(*(LDS_AS float*)(tb + (gi[j] & 0xffffu)), *(LDS_AS float*)(tb + (gi[j] >> 16)));
    st16_wt(rsk, tid * 16, o);
    if (tid < 16) { u32x4 z = {0, 0, 0, 0}; st16_wt(rsk, 4096 + tid * 16, z); }
    else if (tid < 48) { u32x4 z = {~0u, ~0u, ~0u, ~0u}; st16_wt(mkrsrc(a.ws + WS_STAT, (u32)STAT_BYTES), (d * 32 + tid - 16) * 16, z); }
    else if (tid < 56 && d < 512) { u32x4 z = {0, 0, 0, 0}; st16_wt(mkrsrc(a.ws + WS_FLAG, (u32)FLAG_BYTES), (d * 8 + tid - 48) * 16, z); }
  } else if (b < K1_KG + K1_LN) {
    __builtin_amdgcn_s_setprio(3);
    const int lb = b - K1_KG;
    const int rb = (lb >> 4) * 8 + (lb & 7), chalf = (lb >> 3) & 1;
    constexpr int PITCH = 1152 + 32;
    const u32x4* src = (const u32x4*)(a.x + (size_t)rb * 32 * D);
    u32x4 v[9];
#pragma unroll
    for (int i = 0; i < 9; i++) v[i] = __builtin_nontemporal_load(src + tid + 256 * i);
#pragma unroll
    for (int i = 0; i < 9; i++) {
      int c = tid + 256 * i, r = c / 72, cc = c - r * 72;
      *(LDS_AS u32x4*)(smem + r * PITCH + cc * 16) = v[i];
    }
    __syncthreads();
    const int r = tid >> 3, sub = tid & 7;
    u32x4 q[9];
    f32x4 s4v = {0, 0, 0, 0};
#pragma unroll
    for (int i = 0; i < 9; i++) {
      q[i] = *(LDS_AS u32x4*)(smem + r * PITCH + (sub + 8 * i) * 16);
#pragma unroll
      for (int j = 0; j < 4; j++) s4v[j] = dot2(q[i][j], 0x3f803f80u, s4v[j]);
    }
    const float mean = sum8((s4v[0] + s4v[1]) + (s4v[2] + s4v[3])) * (1.0f / D);
    f32x4 qa = {0, 0, 0, 0}, qb = qa;
#pragma unroll
    for (int i = 0; i < 9; i++)
#pragma unroll
      for (int j = 0; j < 4; j++) {
        const float d0 = bflo(q[i][j]) - mean, d1 = bfhi(q[i][j]) - mean;
        qa[j] = fmaf(d0, d0, qa[j]); qb[j] = fmaf(d1, d1, qb[j]);
      }
    qa += qb;
    const float q2 = sum8((qa[0] + qa[1]) + (qa[2] + qa[3]));
    LDS_AS f32x2* st = (LDS_AS f32x2*)(smem + 32 * PITCH);
    if (sub == 0) {
      const float sd = rbf(sqrtf(rbf(rbf(q2 * (1.0f / D)) + 1e-5f)));
      f32x2 o = {rbf(mean), 1.0f / sd};
      st[r] = o;
    }
    __syncthreads();
    const int g = lane >> 4, i16 = lane & 15, q4 = i16 >> 2, p4 = i16 & 3;
    f32x2 rst[8];
#pragma unroll
    for (int j = 0; j < 8; j++) rst[j] = st[8 * g + j];
    auto rsu = mkrsrc(a.ws + WS_UX + (size_t)rb * D * 128, D * 128);
#pragma unroll
    for (int it = 0; it < 5; it++) {
      if (wid + 4 * it >= 18) break;
      const int cg = chalf * 18 + wid + 4 * it;
      LDS_AS char* p0 = smem + (8 * g + q4) * PITCH + cg * 32 + p4 * 8;
      u32x4 xv = tr_read2(p0, p0 + 4 * PITCH);
      u32x4 uv;
#pragma unroll
      for (int j = 0; j < 4; j++) {
        const u32 dpk = pk2(bflo(xv[j]) - rst[2 * j][0], bfhi(xv[j]) - rst[2 * j + 1][0]);
        uv[j] = pk2(bflo(dpk) * rst[2 * j][1], bfhi(dpk) * rst[2 * j + 1][1]);
      }
      const u32 off = (u32)(cg * 16 + i16) * 128 + g * 16;
      st16_wt(rsu, off, uv);
      st16_wt(rsu, off + 64, xv);
    }
  } else {
    __builtin_amdgcn_s_setprio(2);
    const int pb = b - K1_LN - K1_KG;
    u32 cpf = 0;
    if (pb < 40 && wid == 0) {
      u64 pc;
      asm volatile("s_getpc_b64 s[40:41]\n\ts_add_u32 s40, s40, _Z7k2_main6K2Args@rel32@lo+4\n\ts_addc_u32 s41, s41, _Z7k2_main6K2Args@rel32@hi+12\n\ts_mov_b64 %0, s[40:41]"
                   : "=s"(pc) :: "s40", "s41", "scc");
      cpf = *(const volatile u32*)((const char*)pc + (pb >> 3) * 8192 + lane * 128);
    }
    for (int i = 0; i < 3; i++) __builtin_amdgcn_s_sleep(8);
    asm volatile("" ::"v"(cpf));
    const int qi = pb >> 2;
    const int unit = pb < 160 ? (((qi / 5) * 9 + 2 * (qi % 5)) << 2) + (pb & 3) : 288 + 128 + (pb - 160);
    u32x4 v[9];
    retile_load<256, 9>(a.w1, a.w3, a.w2, unit, tid, v);
    retile_stage<256, 9>(smem, unit, tid, v);
    __syncthreads();
    const RtDst rd = retile_read(a.ws, smem, unit, lane, wid, true, v);
    const auto rsw = mkrsrc(rd.base, 36864);
#pragma unroll
    for (int i = 0; i < 9; i++) st16_wt(rsw, rt_off(rd, i) + rd.voff, v[i]);
  }
}

constexpr int CV_CP = 4160;
constexpr int CV_UOFF = 4 * CV_CP;
constexpr int CV_BYTES = CV_UOFF + 4608;
constexpr int CV_K0 = 36864;
constexpr int RA = 0, RB = 36864, RC = 73728, RD = 110592;
constexpr int G1_RING = 36864, G1_ACT = 16384 + 128, G1_STAGE = G1_ACT + 4096;
constexpr int G1_RT = G1_RING + 4 * G1_STAGE;
constexpr int G1_CSOFF = G1_RING + 6 * G1_STAGE;
constexpr int G2_RES = 147456;
constexpr int K2_LDS = G1_CSOFF + 2048;
constexpr int g2_slot(int s) { return (s & 3) == 0 ? RD : ((s & 3) == 1 ? RA : ((s & 3) == 2 ? RB : RC)); }
constexpr int g1_region(int p) { return p == 0 || p == 6 ? RA : (p == 3 || p == 7 ? RB : (p == 4 || p == 8 ? RC : RD)); }
constexpr int FL_HFF = 32768, FL_A = 57344, FL_RT = 61440;

struct K2Args { const u16 *dskip, *w1, *w3, *w2; char* ws; u16* out; };

struct G1Acc { f32x4 v[2][2]; };
struct G1Row { float mu[2], sd2[2], nrl2[2]; };
template <int PANEL>
DEVI void g1_epilogue(const G1Acc& acc, LDS_AS char* smem, int g, const G1Row& r, __amdgpu_buffer_rsrc_t rsh, u32 voff, int ft) {
  LDS_AS float* cs = (LDS_AS float*)(smem + G1_CSOFF) + PANEL * 32 + 4 * g;
  const f32x4 cg = *(LDS_AS f32x4*)cs, cu = *(LDS_AS f32x4*)(cs + 16);
  float o[2][4];
#pragma unroll
  for (int mf = 0; mf < 2; mf++) {
#pragma unroll
    for (int j = 0; j < 4; j++) {
      float tg = fmaf(-r.mu[mf], cg[j], acc.v[0][mf][j]);
      asm("" : "+v"(tg));
      float tu = fmaf(-r.mu[mf], cu[j], acc.v[1][mf][j]);
      asm("" : "+v"(tu));
      const float e = __builtin_amdgcn_exp2f(tg * r.nrl2[mf]);
      const float q = __builtin_amdgcn_rcpf(fmaf(e, r.sd2[mf], r.sd2[mf]));
      float p = tg * tu;
      asm("" : "+v"(p));
      p *= q;
      asm("" : "+v"(p));
      o[mf][j] = p;
    }
  }
  const auto s0 = __builtin_amdgcn_permlane16_swap(pk2(o[0][0], o[0][1]), pk2(o[1][0], o[1][1]), false, false);
  const auto s1 = __builtin_amdgcn_permlane16_swap(pk2(o[0][2], o[0][3]), pk2(o[1][2], o[1][3]), false, false);
  const u32x4 v = {s0[0], s1[0], s0[1], s1[1]};
  const int ftl = ft & 7;
  const int q = PANEL < 8 ? (PANEL >> 1) * 8 + ftl : 32 + (ftl >> 1);
  const int half = PANEL < 8 ? (PANEL & 1) : (ftl & 1);
  st16_wt(rsh, voff + (u32)((ft >> 3) * 36 + q) * 1024 + (u32)half * 512, v);
}

struct G1Bases { LDS_AS char* b[3]; };
template <int OFF>
DEVI u32x4 g1_ldw(const G1Bases& wb) { constexpr int I = OFF / 49152 < 2 ? OFF / 49152 : 2; return *(LDS_AS u32x4*)(wb.b[I] + (OFF - I * 49152)); }

struct G1Ctx {
  const char *hTw, *Wt, *Wq;
  u32 aoff, loff;
  u32 lds0, ldsw, ldsa;
  u32 rot2k;
  int wid;
  G1Bases wb, ab;
  const char *asrc, *wsrc;
};
template <int I>
DEVI void g2_piece(const G1Ctx& c, int s, u32 slot) {
  if constexpr (I < 4) dma16(c.asrc + (size_t)(I >> 1) * 144 * 1024 + s * 2048 + (I & 1) * 1024, c.loff, c.lds0 + slot + c.wid * 4096 + I * 1024);
  else if constexpr (I < 8) dma16(c.wsrc + (size_t)s * 18432 + (I - 4) * 4096, c.loff, c.ldsw + slot + 16384 + (I - 4) * 4096);
  else { if (c.wid < 2) dma16(c.wsrc + (size_t)s * 18432 + 16384, c.loff, c.ldsw + slot + 32768); }
}
DEVI void g2_stage_dma(const G1Ctx& c, int s, u32 slot) {
  g2_piece<0>(c, s, slot); g2_piece<1>(c, s, slot); g2_piece<2>(c, s, slot); g2_piece<3>(c, s, slot); g2_piece<4>(c, s, slot);
  g2_piece<5>(c, s, slot); g2_piece<6>(c, s, slot); g2_piece<7>(c, s, slot); g2_piece<8>(c, s, slot);
}
DEVI u32 g1_rot(const G1Ctx& c, u32 off) { const u32 x = off + c.rot2k; return x >= 36864u ? x - 36864u : x; }
template <int S, int I>
DEVI void g1_piece(const G1Ctx& c) {
  const u32 po = g1_rot(c, S * 2048);
  constexpr u32 dst = G1_RING + (S % 6) * G1_STAGE;
  if constexpr (I < 2) dma16(c.hTw + I * 16 * (L * 2), c.aoff + po * 64u, c.ldsa + dst + I * 8256);
  else { if ((c.wid >> 2) == (S & 1)) dma16(c.Wq, c.loff + po, c.ldsw + dst + G1_ACT - (S & 1) * 4096); }
}
template <int S> DEVI void g1_stage3(const G1Ctx& c) { g1_piece<S, 0>(c); g1_piece<S, 1>(c); g1_piece<S, 2>(c); }
template <int P>
DEVI void g1_frag(const G1Ctx& c, int fr) {
  fr = fr > 35 ? 35 : fr;
  dma16(c.Wt + P * 36864, c.loff + g1_rot(c, fr * 1024), c.lds0 + g1_region(P) + fr * 1024);
}
template <int PANEL, int I>
DEVI void g1_refill(const G1Ctx& c) {
  if constexpr (PANEL == 3) { if constexpr (I < 2) g1_frag<5>(c, 24 + 8 * I + c.wid); else if constexpr (I < 7) g1_frag<6>(c, 8 * (I - 2) + c.wid); }
  else if constexpr (PANEL < 6) {
    if constexpr (I < 5) g1_frag<PANEL + 3>(c, 8 * I + c.wid);
  }
  else { if (c.wid < 4) g2_piece<I>(c, PANEL - 6, g2_slot(PANEL - 6)); }
}

template <int PANEL, int K>
struct G1PassStep {
  static DEVI void run(G1Acc& acc, u32x4 (&w)[3], const u32x4 (&act)[36], const G1Ctx& c) {
    u32x4 cur = w[K % 3];
    acc.v[K & 1][0] = mfma16(cur, act[K & ~1], acc.v[K & 1][0]);
    acc.v[K & 1][1] = mfma16(cur, act[K | 1], acc.v[K & 1][1]);
    OPAQUE(acc.v[K & 1][0]); OPAQUE(acc.v[K & 1][1]);
    if constexpr (K + 3 < 36) w[K % 3] = g1_ldw<g1_region(PANEL) + (K + 3) * 1024>(c.wb);
    if constexpr (K % 4 == 1) g1_refill<PANEL, K / 4>(c);
    PIN();
    if constexpr (K + 1 < 36) G1PassStep<PANEL, K + 1>::run(acc, w, act, c);
  }
};
template <int PANEL>
DEVI G1Acc g1_pass(const u32x4 (&act)[36], const G1Ctx& c) {
  G1Acc acc = {};
  u32x4 w[3];
  constexpr int R = g1_region(PANEL);
  w[0] = g1_ldw<R>(c.wb); w[1] = g1_ldw<R + 1024>(c.wb); w[2] = g1_ldw<R + 2048>(c.wb);
  PIN();
  G1PassStep<PANEL, 0>::run(acc, w, act, c);
  return acc;
}

template <int J>
DEVI u32x4 g1_lda(const G1Ctx& c) {
  constexpr int OFF = G1_RING + ((J >> 1) % 6) * G1_STAGE + (J & 1) * 32, I = OFF / 49152 < 2 ? OFF / 49152 : 2;
  LDS_AS char* p = c.ab.b[I] + (OFF - I * 49152);
  return tr_read2(p, p + 2048);
}
template <int J, int P>
DEVI u32x4 g1_ldw1(const G1Ctx& c) {
  if constexpr (P == 0) return g1_ldw<RA + J * 1024>(c.wb);
  else return g1_ldw<G1_RING + ((J >> 1) % 6) * G1_STAGE + G1_ACT + (P - 1) * 2048 + (J & 1) * 1024>(c.wb);
}
DEVI void flags_add(const char* fl, int b) { __hip_atomic_fetch_add((u32*)(fl + (b & 15) * 128), 1u, __ATOMIC_RELAXED, __HIP_MEMORY_SCOPE_AGENT); }
DEVI bool flags_set(u32 v) { return __all(v >= 16u); }
DEVI void flags_spin(const char* fl, u32 foff) {
  for (u32 spins = 0; spins < (1u << 22); ++spins) {
    u32 v;
    asm volatile("global_load_dword %0, %1, %2 sc1\n\ts_waitcnt vmcnt(0)" : "=&v"(v) : "v"(foff), "s"(fl) : "memory");
    if (flags_set(v)) break;
    __builtin_amdgcn_s_sleep(4);
  }
}

template <int N> DEVI void waitv() { asm volatile("s_waitcnt vmcnt(%0)" ::"i"(N) : "memory"); }

struct G1Stat {
  const char *stat, *flag, *csp;
  u32 soff, coff, foff; int ft, b;
  u32x2 g[3]; u32 fl; u32x4 cs;
  RtDst rd;
};

template <int J>
struct G1P1Step {
  static DEVI void run(const G1Ctx& c, G1Acc (&acc)[4], u32x4 (&w)[3], u32x4 (&act)[36], G1Stat& st, const u32x4 (&rf)[9], int lane) {
    constexpr int S = J >> 1;
    if constexpr (J & 1) {
      if constexpr (S < 3) { if (c.wid >= 4) waitv<(S == 0 ? 15 : (S == 1 ? 10 : 7))>(); }
      else if constexpr (S <= 8) { if (c.wid >= 4) waitv<6>(); else waitv<9>(); }
      else if constexpr (S <= 13) waitv<6>();
      else if constexpr (S < 17) waitv<S - 4>();
      else waitv<5>();
      BAR();
      if constexpr (S == 11) { if (c.wid == 0 && lane == 0) flags_add(st.flag + FL_RT, st.b); }
      if constexpr (S == 13) {
#pragma unroll
        for (int i = 0; i < 3; i++)
          asm volatile("global_load_dwordx2 %0, %1, %2 sc1" : "=v"(st.g[i]) : "v"(st.soff), "s"(st.stat + i * 2048) : "memory");
        asm volatile("global_load_dwordx4 %0, %1, %2 sc1" : "=v"(st.cs) : "v"(st.coff), "s"(st.csp) : "memory");
      }
      if constexpr (S == 15) asm volatile("global_load_dword %0, %1, %2 sc1" : "=v"(st.fl) : "v"(st.foff), "s"(st.flag + FL_RT) : "memory");
      if constexpr (S == 17) {
        asm volatile("" : "+v"(st.fl));
        if (!flags_set(st.fl)) flags_spin(st.flag + FL_RT, st.foff);
      }
    }
    if constexpr ((J & 1) && J + 1 < 36) { act[J + 1] = g1_lda<J + 1>(c); act[J + 2] = g1_lda<J + 2>(c); }
    PIN();
#define G1_ONE(P)                                              \
    { u32x4 cur = w[P];                                        \
      acc[P].v[J & 1][0] = mfma16(cur, act[J & ~1], acc[P].v[J & 1][0]);   \
      acc[P].v[J & 1][1] = mfma16(cur, act[J | 1], acc[P].v[J & 1][1]);    \
      OPAQUE(acc[P].v[J & 1][0]); OPAQUE(acc[P].v[J & 1][1]);  \
      if constexpr (J + 1 < 36) w[P] = g1_ldw1<J + 1, P>(c);   \
      PIN(); }
    constexpr int TP = 3 + (S - 13) / 2, TF = S == 14 ? 16 : (S == 16 ? 24 : 0);
    constexpr bool T3 = S == 14 || S == 15 || S == 17;
    G1_ONE(0)
    if constexpr ((J & 1) && S > 0) { if constexpr (S < 13) g1_piece<S + 5, 0>(c); else g1_frag<TP>(c, TF + c.wid); PIN(); }
    if constexpr (!(J & 1) && S < 9) {
      if (c.wid < 4) asm volatile("global_store_dwordx4 %0, %1, %2 sc1\n\ts_nop 1" ::"v"(st.rd.voff), "v"(rf[S]), "s"(st.rd.base + rt_off(st.rd, S)) : "memory");
      PIN();
    }
    if constexpr (J == 6) {
      if (st.ft < 18 && st.ft % 3 == 0) {
        float s1[2] = {0.f, 0.f}, s2[2] = {0.f, 0.f};
#pragma unroll
        for (int k = 0; k < 6; k++)
#pragma unroll
          for (int j = 0; j < 4; j++) { s1[k & 1] = dot2(act[k][j], 0x3f803f80u, s1[k & 1]); s2[k & 1] = dot2(act[k][j], act[k][j], s2[k & 1]); }
#pragma unroll
        for (int i = 0; i < 2; i++) {
          s1[i] += __shfl_xor(s1[i], 16); s1[i] += __shfl_xor(s1[i], 32);
          s2[i] += __shfl_xor(s2[i], 16); s2[i] += __shfl_xor(s2[i], 32);
        }
        u32x2 o = {__float_as_uint((lane & 16) ? s1[1] : s1[0]), __float_as_uint((lane & 16) ? s2[1] : s2[0])};
        if (o.y == ~0u) o.y = 0x7fc00000u;
        asm volatile("global_store_dwordx2 %0, %1, %2 sc1\n\ts_nop 1" ::"v"(st.soff & 2047u), "v"(o), "s"(st.stat + (st.ft / 3) * 2048) : "memory");
      }
      PIN();
    }
    G1_ONE(1)
    if constexpr (!(J & 1) && S - 1 > 0 && S - 1 < 13) { g1_piece<S + 4, 2>(c); PIN(); }
    if constexpr ((J & 1) && T3) { g1_frag<TP>(c, TF + 16 + c.wid); PIN(); }
    G1_ONE(2)
    if constexpr ((J & 1) && S > 0) { if constexpr (S < 13) g1_piece<S + 5, 1>(c); else g1_frag<TP>(c, TF + 8 + c.wid); PIN(); }
#undef G1_ONE
    if constexpr (J + 1 < 36) G1P1Step<J + 1>::run(c, acc, w, act, st, rf, lane);
  }
};

template <int SLOT_N, int KK_N, bool SYNC>
DEVI void g2_step(f32x4 (&acc)[2][9], const u32x4 (&A)[2], const u32x4 (&W)[9], u32x4 (&An)[2], u32x4 (&Wn)[9],
                  const G1Bases& ab, const G1Bases& wb) {
#define G2_MM(nf) acc[0][nf] = mfma16(W[nf], A[0], acc[0][nf]); acc[1][nf] = mfma16(W[nf], A[1], acc[1][nf]); \
                  OPAQUE(acc[0][nf]); OPAQUE(acc[1][nf]); PIN();
#define G2_LW(nf) Wn[nf] = g1_ldw<SLOT_N + 16384 + KK_N * 9216 + (nf) * 1024>(wb);
  G2_MM(0)
  An[0] = g1_ldw<SLOT_N + KK_N * 1024>(ab); An[1] = g1_ldw<SLOT_N + 2048 + KK_N * 1024>(ab); PIN();
  G2_MM(1) G2_LW(0) G2_LW(1) PIN();
  G2_MM(2) G2_LW(2) G2_LW(3) PIN();
  G2_MM(3) G2_LW(4) G2_LW(5) PIN();
  G2_MM(4) G2_LW(6) G2_LW(7) PIN();
  G2_MM(5) G2_LW(8) PIN();
  G2_MM(6)
  G2_MM(7)
  if constexpr (SYNC) { WAITL(0); BAR(); PIN(); }
  G2_MM(8)
#undef G2_MM
#undef G2_LW
}
template <int S4, bool LAST>
DEVI void g2_stage(f32x4 (&o)[2][9], u32x4 (&A0)[2], u32x4 (&W0)[9], u32x4 (&A1)[2], u32x4 (&W1)[9], const G1Bases& ab, const G1Bases& wb) {
  g2_step<g2_slot(S4), 1, !LAST>(o, A0, W0, A1, W1, ab, wb);
  g2_step<g2_slot(LAST ? S4 : S4 + 1), 0, false>(o, A1, W1, A0, W0, ab, wb);
}

template <int E0, int NE>
DEVI void cv_copies(LDS_AS char* base, int tt, const u32x4& k0, const u32x4& k1) {
  const u32 w[8] = {k0.x, k0.y, k0.z, k0.w, k1.x, k1.y, k1.z, k1.w};
#pragma unroll
  for (int e = E0; e < E0 + NE; e++) {
    u32x4 o;
#pragma unroll
    for (int j = 0; j < 4; j++)
      o[j] = (e & 1) ? __builtin_amdgcn_alignbyte(w[(e >> 1) + j + 1], w[(e >> 1) + j], 2) : w[(e >> 1) + j];
    *(LDS_AS u32x4*)(base + e * CV_CP + tt * 16) = o;
  }
}
DEVI u32x4 cv_lda(LDS_AS char* p) {
  const u32x2 lo = *(volatile LDS_AS u32x2*)p, hi = *(volatile LDS_AS u32x2*)(p + 8);
  return (u32x4){lo.x, lo.y, hi.x, hi.y};
}
template <int NW, int STRIDE>
DEVI void cv_sum(LDS_AS char* p, f32x4& y0, f32x4& y1) {
  y0 = (f32x4){0, 0, 0, 0}; y1 = y0;
#pragma unroll
  for (int w = 0; w < NW; w++) { y0 += *(LDS_AS f32x4*)(p + w * STRIDE); y1 += *(LDS_AS f32x4*)(p + w * STRIDE + 256); }
}
DEVI u32x4 cv_out(const f32x4& y0, const f32x4& y1, const u32x4& uv, const u32x4& xv, float dsk) {
  const float y[8] = {y0[0], y0[1], y0[2], y0[3], y1[0], y1[1], y1[2], y1[3]};
  u32x4 o;
#pragma unroll
  for (int j = 0; j < 4; j++) {
    const u32 yb = pk2(y[2 * j], y[2 * j + 1]), sk = pk2(bflo(uv[j]) * dsk, bfhi(uv[j]) * dsk);
    const u32 z = pk2(bflo(yb) + bflo(sk), bfhi(yb) + bfhi(sk));
    o[j] = pk2(bflo(xv[j]) + bflo(z), bfhi(xv[j]) + bfhi(z));
  }
  return o;
}

__global__ __launch_bounds__(512, 2) void k2_main(K2Args a) {
  extern __shared__ __attribute__((aligned(16))) char smem_[];
  LDS_AS char* smem = (LDS_AS char*)smem_;
  const int tid = threadIdx.x, lane = tid & 63;
  const int wid = __builtin_amdgcn_readfirstlane(tid >> 6);
  const int b = blockIdx.x, xcd = b & 7, rr = b >> 3;
  const int mt = (xcd & 1) * 4 + (rr & 3), ft = (xcd >> 1) * 8 + (rr >> 2);
  G1Ctx c;
  c.wid = wid;
  c.rot2k = (ft < 18 ? ft : ft - 18) * 2048;
  c.lds0 = (u32)(size_t)smem;
  c.ldsw = c.lds0 + wid * 1024;
  c.ldsa = c.ldsw + (wid >> 2) * 32;
  c.hTw = a.ws + WS_HT + mt * 512 + (size_t)(2 * wid) * (L * 2);
  c.Wt = a.ws + WS_WT + (size_t)ft * 9 * 36864;
  c.Wq = c.Wt + (1 + ((wid >> 1) & 1)) * 36864 + (wid & 1) * 1024;
  c.loff = lane * 16;
  const int arow = lane >> 5;
  c.aoff = arow * (L * 2) + (((lane & 31) ^ (((2 * wid + arow) & 3) << 2)) * 16);
  const int unit = b < 128 ? ((((b >> 4) * 9 + 2 * ((b >> 2) & 3) + 1)) << 2) + (b & 3) : 288 + (b - 128);
  u32x4 rv[5], rf[9];
  RtDst rd;
  {
    const int half = wid >> 2, wl = wid & 3, tt = tid & 255;
    const int dm = b + 256 * half;
    const int dc = 512 + (b >> 2), nlo = b & 3, nhi = 7 - nlo;
    const u32x4* krm = (const u32x4*)(a.ws + WS_KREV + (size_t)dm * KREV_PITCH);
    const u32x4* krc = (const u32x4*)(a.ws + WS_KREV + (size_t)dc * KREV_PITCH);
    const char* ux = a.ws + WS_UX + (size_t)(tt >> 2) * D * 128 + (tt & 3) * 16;
    const u32x4 k0 = krm[tt], k1 = krm[tt + 1], uv = *(const u32x4*)(ux + dm * 128);
    const u32x4 kc0 = krc[tt], kc1 = krc[tt + 1], uc = *(const u32x4*)(ux + dc * 128);
    const u32x4 xv = *(const u32x4*)(ux + dm * 128 + 64);
    const int te = ((tt & 32) ? nlo : nhi) * 32 + (tt & 31);
    const char* uxe = a.ws + WS_UX + ((size_t)(te >> 2) * D + dc) * 128 + (te & 3) * 16;
    const u32x4 ue = *(const u32x4*)uxe, xe = *(const u32x4*)(uxe + 64);
    const float dskm = bf2f(a.dskip[dm]), dskc = bf2f(a.dskip[dc]);
    const u32x4* rp[5];
    retile_addr<512, 5>(a.w1, a.w3, a.w2, unit, tid, rp);
#pragma unroll
    for (int i = 0; i < 5; i++) OPAQUE(rp[i]);
    LDS_AS char* cm = smem + CV_K0 + half * CV_BYTES;
    LDS_AS char* cc = smem + CV_K0 + 2 * CV_BYTES;
    cv_copies<0, 4>(cm, tt, k0, k1);
    *(LDS_AS u32x4*)(cm + CV_UOFF + 512 + tt * 16) = uv;
    if (half == 0) cv_copies<0, 2>(cc, tt, kc0, kc1);
    else { cv_copies<2, 2>(cc, tt, kc0, kc1); *(LDS_AS u32x4*)(cc + CV_UOFF + 512 + tt * 16) = uc; }
    {
      const u32x4 z = {0, 0, 0, 0};
      if (tid == 511) *(LDS_AS u32*)(smem + G1_CSOFF) = 0u;
      if (tid < 144) {
        const int ch = tid / 48, i = tid - ch * 48;
        *(LDS_AS u32x4*)(smem + CV_K0 + ch * CV_BYTES + (i < 16 ? (i >> 2) * CV_CP + 4096 + (i & 3) * 16 : CV_UOFF + (i - 16) * 16)) = z;
      }
    }
    __builtin_amdgcn_s_waitcnt(0x0070);
    BAR();
    const int i16 = lane & 15, g = lane >> 4;
    const int aoff = ((3 - i16) & 3) * CV_CP + (2028 - (i16 & 12) + 8 * g) * 2;
    const int boff = CV_UOFF + (2 * i16 + g + 30) * 16;
    f32x4 acc[8], eh = {0, 0, 0, 0}, el = eh;
#pragma unroll
    for (int n = 0; n < 8; n++) acc[n] = (f32x4){0, 0, 0, 0};
    {
      LDS_AS char* ap = cm + aoff - 128 * wl;
      LDS_AS char* bp = cm + boff - 128 * wl;
      LDS_AS char* apc = cc + aoff - 64 * wid;
      LDS_AS char* bh = cc + boff - 64 * wid + 512 * nhi;
      LDS_AS char* bl = cc + boff - 64 * wid + 512 * nlo;
      u32x4 A[2][8], B[2][8], Ac[8], Bh[8], Bl[4];
      A[0][0] = cv_lda(ap);
#pragma unroll
      for (int j = 0; j < 8; j++) B[0][j] = *(LDS_AS u32x4*)(bp + 512 * j);
      A[0][1] = cv_lda(ap - 512);
      PIN();
#pragma unroll
      for (int a1 = 0; a1 < 8; a1++) {
        if (a1 + 2 < 8) A[0][a1 + 2] = cv_lda(ap - 512 * (a1 + 2));
        B[1][a1] = *(LDS_AS u32x4*)(bp - 64 + 512 * a1);
        if (a1 >= 6) A[1][a1 - 6] = cv_lda(ap - 64 - 512 * (a1 - 6));
        PIN();
#pragma unroll
        for (int n = a1; n < 8; n++) acc[n] = mfma16(A[0][a1], B[0][n - a1], acc[n]);
        if (a1 < 5) rv[a1] = __builtin_nontemporal_load(rp[a1]);
        PIN();
      }
#pragma unroll
      for (int a1 = 0; a1 < 8; a1++) {
        if (a1 + 2 < 8) A[1][a1 + 2] = cv_lda(ap - 64 - 512 * (a1 + 2));
        Ac[a1] = cv_lda(apc - 512 * a1);
        Bh[a1] = *(LDS_AS u32x4*)(bh - 512 * (a1 <= nhi ? a1 : nhi));
        if (a1 < 4) Bl[a1] = *(LDS_AS u32x4*)(bl - 512 * (a1 <= nlo ? a1 : nlo));
        PIN();
#pragma unroll
        for (int n = a1; n < 8; n++) acc[n] = mfma16(A[1][a1], B[1][n - a1], acc[n]);
        PIN();
      }
#pragma unroll
      for (int a1 = 0; a1 < 8; a1++) {
        if (a1 <= nhi) eh = mfma16(Ac[a1], Bh[a1], eh);
        if (a1 < 4 && a1 <= nlo) el = mfma16(Ac[a1], Bl[a1], el);
      }
    }
    WAITL(0); BAR();
    LDS_AS char* pm = smem + CV_K0 + half * 32768;
    LDS_AS char* pc = smem + CV_K0 + 65536;
#pragma unroll
    for (int n = 0; n < 8; n++) *(LDS_AS f32x4*)(pm + wl * 8192 + n * 1024 + lane * 16) = acc[n];
    *(LDS_AS f32x4*)(pc + wid * 2048 + lane * 16) = eh;
    *(LDS_AS f32x4*)(pc + wid * 2048 + 1024 + lane * 16) = el;
    WAITL(0); BAR();
    {
      const int soff = ((tt >> 5) * 64 + ((tt & 31) >> 1) + 32 * (tt & 1)) * 16;
      f32x4 y0, y1;
      cv_sum<4, 8192>(pm + soff, y0, y1);
      st16_wt(mkrsrc(a.ws + WS_HT + (size_t)dm * (L * 2), L * 2), tt * 16, cv_out(y0, y1, uv, xv, dskm));
      if (wid == 4) {
        cv_sum<8, 2048>(pc + soff, y0, y1);
        st16_wt(mkrsrc(a.ws + WS_HT + (size_t)dc * (L * 2), L * 2), te * 16, cv_out(y0, y1, ue, xe, dskc));
      }
    }
    retile_stage<512, 5>(smem + G1_RT, unit, tid, rv);
    WAITV(0);
    __builtin_amdgcn_s_waitcnt(0x0070);
    BAR();
    if (tid == 0) flags_add(a.ws + WS_FLAG + FL_A, b);
    rd = retile_read(a.ws, smem + G1_RT, unit, lane, wl, wid < 4, rf);
    WAITL(0);
    if (wid >= 4) {
      volatile LDS_AS u32* go = (volatile LDS_AS u32*)(smem + G1_CSOFF);
      if (wid == 4) { __builtin_amdgcn_s_sleep(8); flags_spin(a.ws + WS_FLAG + FL_A, (lane & 15) * 128); *go = 1u; }
      else {
#pragma unroll 1
        for (int i = wid - 5; i < 36; i += 3) g1_frag<0>(c, i);
        for (u32 spins = 0; *go == 0u && spins < (1u << 24); ++spins) __builtin_amdgcn_s_sleep(1);
      }
#pragma unroll 1
      for (int S = 0; S < 4; S++) {
        const u32 po = g1_rot(c, S * 2048), dst = c.ldsw - 4096 + G1_RING + S * G1_STAGE;
#pragma unroll
        for (int i = 0; i < 4; i++) dma16(c.hTw + (i - 1) * 8 * (L * 2), c.aoff + po * 64u, dst + i * 4128);
        dma16(c.Wq, c.loff + po, dst + G1_ACT);
      }
      WAITV(15);
    }
  }
  const int hh = lane >> 5, g4 = lane >> 4, q4 = (lane & 15) >> 2, p4 = lane & 3;
#pragma unroll
  for (int i = 0; i < 3; i++) {
    c.ab.b[i] = smem + (8 * g4 + q4) * 512 + g4 * 32 + ((wid ^ q4) * 64) + p4 * 8 + i * 49152;
    c.wb.b[i] = smem + lane * 16 + i * 49152;
    OPAQUE(c.ab.b[i]); OPAQUE(c.wb.b[i]);
  }
  G1Stat st;
  st.stat = a.ws + WS_STAT + (size_t)mt * 18 * 2048;
  st.flag = a.ws + WS_FLAG;
  st.csp = a.ws + WS_CSP + ft * 8192;
  st.soff = hh * 3 * 2048 + (wid * 32 + (lane & 31)) * 8;
  st.coff = tid * 16;
  st.foff = (lane & 15) * 128;
  st.ft = ft; st.b = b;
  u32x4 act[36], w[3];
  G1Acc acc[4] = {};
  BAR();
  g1_stage3<4>(c); g1_stage3<5>(c);
  act[0] = g1_lda<0>(c); act[1] = g1_lda<1>(c);
  w[0] = g1_ldw1<0, 0>(c); w[1] = g1_ldw1<0, 1>(c); w[2] = g1_ldw1<0, 2>(c);
  PIN();
  st.rd = rd;
  G1P1Step<0>::run(c, acc, w, act, st, rf, lane);
  asm volatile("" : "+v"(st.g[0]), "+v"(st.g[1]), "+v"(st.g[2]), "+v"(st.cs));
  *(LDS_AS float*)(smem + G1_CSOFF + tid * 4) = (__uint_as_float(st.cs[0]) + __uint_as_float(st.cs[1])) + (__uint_as_float(st.cs[2]) + __uint_as_float(st.cs[3]));
  {
    bool bad = st.g[0].y == ~0u || st.g[1].y == ~0u || st.g[2].y == ~0u;
    for (u32 spins = 0; __any(bad) && spins < (1u << 22); ++spins) {
      __builtin_amdgcn_s_sleep(2);
      bad = false;
#pragma unroll
      for (int i = 0; i < 3; i++) {
        asm volatile("global_load_dwordx2 %0, %1, %2 sc1\n\ts_waitcnt vmcnt(0)" : "=&v"(st.g[i]) : "v"(st.soff), "s"(st.stat + i * 2048) : "memory");
        bad |= (st.g[i].y == ~0u);
      }
    }
  }
  float s1 = 0.f, s2 = 0.f;
#pragma unroll
  for (int i = 0; i < 3; i++) { s1 += __uint_as_float(st.g[i].x); s2 += __uint_as_float(st.g[i].y); }
  s1 += __shfl_xor(s1, 32); s2 += __shfl_xor(s2, 32);
  const float mean = s1 * (1.0f / D);
  const float var = fmaxf(s2 * (1.0f / D) - mean * mean, 0.f);
  const float mu = rbf(mean);
  const float sd = rbf(sqrtf(rbf(rbf(var) + 1e-5f)));
  const float mu2 = __shfl_xor(mu, 16), sdo = __shfl_xor(sd, 16);
  G1Row row;
  row.mu[0] = (lane & 16) ? mu2 : mu; row.mu[1] = (lane & 16) ? mu : mu2;
  const float sdA = (lane & 16) ? sdo : sd, sdB = (lane & 16) ? sd : sdo;
  row.sd2[0] = sdA * sdA; row.sd2[1] = sdB * sdB;
  row.nrl2[0] = -1.44269504088896341f / sdA; row.nrl2[1] = -1.44269504088896341f / sdB;
  auto rsh = mkrsrc(a.ws + WS_HFF, (u32)HFF_BYTES);
  const u32 voff = (u32)(mt * 16 + wid * 2 + (g4 & 1)) * (144 * 1024) + (u32)((lane & 15) + 16 * hh) * 16;

  const int ks = xcd >> 1, mt16 = (xcd & 1) * 8 + (rr & 7), nt = rr >> 3;
  const int tile = mt16 * 4 + nt;
  c.asrc = a.ws + WS_HFF + ((size_t)(mt16 * 8 + 2 * (wid & 3)) * 144 + ks * 36) * 1024;
  c.wsrc = a.ws + WS_W2T + ((size_t)nt * 144 + ks * 36) * 9216 + (wid & 3) * 1024;
  u32* fl_out = (u32*)(a.ws + WS_FLAG + FL_HFF + (size_t)(((mt * 2 + (wid >> 2)) * 4 + (ft >> 3)) * 3) * 128);
  const char* fl_in = a.ws + WS_FLAG + FL_HFF + (size_t)((mt16 * 4 + ks) * 3) * 128;
  auto publish = [&](int f) { if (lane == 0) __hip_atomic_fetch_add(fl_out + f * 32, 1u, __ATOMIC_RELAXED, __HIP_MEMORY_SCOPE_AGENT); };
  u32 pollv;
  auto poll_issue = [&](int f) { asm volatile("global_load_dword %0, %1, %2 sc1" : "=v"(pollv) : "v"(0u), "s"(fl_in + f * 128) : "memory"); };
  auto poll_check = [&](int f) {
    asm volatile("" : "+v"(pollv));
    for (u32 spins = 0; __builtin_amdgcn_readfirstlane(pollv) < 32u && spins < (1u << 22); ++spins) {
      __builtin_amdgcn_s_sleep(2);
      asm volatile("global_load_dword %0, %1, %2 sc1\n\ts_waitcnt vmcnt(0)" : "=&v"(pollv) : "v"(0u), "s"(fl_in + f * 128) : "memory");
    }
  };

#define EPI(P, A) g1_epilogue<P>(A, smem, g4, row, rsh, voff, ft); PIN()
  const bool early = wid >= 4;
  WAITL(0); BAR();
  asm volatile("global_load_dwordx4 %0, %1, %2 sc1" : "=v"(st.cs) : "v"(st.coff), "s"(st.csp) : "memory");
  if (early) { EPI(0, acc[0]); EPI(1, acc[1]); }
  acc[3] = g1_pass<3>(act, c);
  if (!early) { EPI(0, acc[0]); EPI(1, acc[1]); }
  WAITV(9);
  asm volatile("" : "+v"(st.cs));
  if (tid >= 160) *(LDS_AS float*)(smem + G1_CSOFF + tid * 4) = (__uint_as_float(st.cs[0]) + __uint_as_float(st.cs[1])) + (__uint_as_float(st.cs[2]) + __uint_as_float(st.cs[3]));
  WAITL(0); BAR();
  if (early) { EPI(2, acc[2]); EPI(3, acc[3]); }
  acc[0] = g1_pass<4>(act, c);
  if (!early) { EPI(2, acc[2]); EPI(3, acc[3]); }
  WAITV(7);
  publish(0);
  BAR();
  if (early) { EPI(4, acc[0]); }
  acc[1] = g1_pass<5>(act, c);
  if (!early) { poll_issue(0); EPI(4, acc[0]); EPI(5, acc[1]); WAITV(2); poll_check(0); }
  else WAITV(7);
  BAR();
  if (early) { EPI(5, acc[1]); }
  acc[2] = g1_pass<6>(act, c);
  if (!early) { EPI(6, acc[2]); } else WAITV(1);
  BAR();
  if (early) { EPI(6, acc[2]); }
  acc[3] = g1_pass<7>(act, c);
  if (!early) { EPI(7, acc[3]); WAITV(9); } else WAITV(1);
  publish(1);
  BAR();
  if (early) { EPI(7, acc[3]); }
  acc[0] = g1_pass<8>(act, c);
  EPI(8, acc[0]);
#undef EPI
  BAR();

  u32* cnt_in = (u32*)(a.ws + WS_FLAG + (size_t)(tile * 4 + ks) * 128);
  if (!early) {
    poll_issue(1);
    g2_stage_dma(c, 3, g2_slot(3));
    {
      const char* hsrc = a.ws + WS_HT + (size_t)(nt * 144) * (L * 2) + (mt16 * 128 + ks * 32) * 2;
      const u32 hoff = (lane >> 2) * (L * 2) + (lane & 3) * 16;
#pragma unroll
      for (int i = 0; i < 3; i++) { const int pc = (wid + 4 * i < 9) ? wid + 4 * i : wid; dma16(hsrc + (size_t)pc * 16 * (L * 2), hoff, c.lds0 + G2_RES + pc * 1024); }
    }
    WAITV(11);
    poll_check(1);
    publish(2);
    BAR();
    g2_stage_dma(c, 4, g2_slot(0));
    BAR();
    g2_stage_dma(c, 5, g2_slot(1));
#define G2_LD(s) { if (wid < 2) WAITV(18); else WAITV(16); BAR(); g2_stage_dma(c, (s) + 4, g2_slot(s)); }
    G2_LD(2) G2_LD(3) G2_LD(4)
    poll_issue(2);
    G2_LD(5) G2_LD(6) G2_LD(7)
    poll_check(2);
    G2_LD(8) G2_LD(9) G2_LD(10) G2_LD(11) G2_LD(12) G2_LD(13)
#undef G2_LD
    if (wid < 2) WAITV(18); else WAITV(16);
    BAR();
    WAITV(8); BAR();
    WAITV(0); BAR();
  } else {
    const int mw = wid - 4;
    G1Bases ab;
#pragma unroll
    for (int i = 0; i < 3; i++) { ab.b[i] = smem + mw * 4096 + lane * 16 + i * 49152; OPAQUE(ab.b[i]); }
    f32x4 o[2][9];
#pragma unroll
    for (int i = 0; i < 2; i++)
#pragma unroll
      for (int j = 0; j < 9; j++) { o[i][j] = (f32x4){0, 0, 0, 0}; OPAQUE(o[i][j]); }
    u32x4 A0[2], W0[9], A1[2], W1[9];
    constexpr int S0 = g2_slot(0);
    A0[0] = g1_ldw<S0>(ab); A0[1] = g1_ldw<S0 + 2048>(ab);
    W0[0] = g1_ldw<S0 + 16384>(c.wb); W0[1] = g1_ldw<S0 + 17408>(c.wb); W0[2] = g1_ldw<S0 + 18432>(c.wb); W0[3] = g1_ldw<S0 + 19456>(c.wb);
    W0[4] = g1_ldw<S0 + 20480>(c.wb); W0[5] = g1_ldw<S0 + 21504>(c.wb); W0[6] = g1_ldw<S0 + 22528>(c.wb); W0[7] = g1_ldw<S0 + 23552>(c.wb);
    W0[8] = g1_ldw<S0 + 24576>(c.wb);
    PIN();
#pragma unroll 1
    for (int it = 0; it < 4; it++) {
      g2_stage<0, false>(o, A0, W0, A1, W1, ab, c.wb);
      g2_stage<1, false>(o, A0, W0, A1, W1, ab, c.wb);
      if (it == 0) { WAITV(0); publish(2); }
      g2_stage<2, false>(o, A0, W0, A1, W1, ab, c.wb);
      g2_stage<3, false>(o, A0, W0, A1, W1, ab, c.wb);
    }
    g2_stage<0, false>(o, A0, W0, A1, W1, ab, c.wb);
    g2_stage<1, true>(o, A0, W0, A1, W1, ab, c.wb);
    if (mw != ks) {
      const auto rs_out = mkrsrc(a.ws + WS_PART + ((size_t)(tile * 4 + mw) * 4 + ks) * 9216, 9216);
#pragma unroll
      for (int j = 0; j < 9; j++) {
        u32x4 v = {pk2(o[0][j][0], o[0][j][1]), pk2(o[0][j][2], o[0][j][3]), pk2(o[1][j][0], o[1][j][1]), pk2(o[1][j][2], o[1][j][3])};
        st16_wt(rs_out, (j * 64 + lane) * 16, v);
      }
      WAITV(0);
      if (lane == 0) __hip_atomic_fetch_add((u32*)(a.ws + WS_FLAG + (size_t)(tile * 4 + mw) * 128), 1u, __ATOMIC_RELAXED, __HIP_MEMORY_SCOPE_AGENT);
    } else {
#pragma unroll
      for (int j = 0; j < 9; j++) {
        *(LDS_AS f32x4*)(smem + g2_slot(2) + ((j * 2) * 64 + lane) * 16) = o[0][j];
        *(LDS_AS f32x4*)(smem + g2_slot(2) + ((j * 2 + 1) * 64 + lane) * 16) = o[1][j];
      }
      WAITL(0);
    }
  }
  BAR();
  {
    u32 spins = 0;
    while (__builtin_amdgcn_readfirstlane(__hip_atomic_load(cnt_in, __ATOMIC_RELAXED, __HIP_MEMORY_SCOPE_AGENT)) < 3u) {
      __builtin_amdgcn_s_sleep(1);
      if (++spins > (1u << 24)) break;
    }
    __builtin_amdgcn_fence(__ATOMIC_ACQUIRE, "wavefront");
    const auto rs_in = mkrsrc(a.ws + WS_PART + (size_t)(tile * 4 + ks) * 4 * 9216, 4 * 9216);
    int ln = threadIdx.x; asm volatile("" : "+v"(ln)); ln &= 63;
    const int lane = ln, g = ln >> 4, q4 = (ln & 15) >> 2, p4 = ln & 3;
    u32x4 pq[2][3];
#pragma unroll
    for (int k = 0; k < 2; k++)
#pragma unroll
      for (int sidx = 0; sidx < 3; sidx++)
        if (k == 0 || wid == 0)
          pq[k][sidx] = __builtin_amdgcn_raw_buffer_load_b128(rs_in, ((ks + 1 + sidx) & 3) * 9216 + ((wid + 8 * k) * 64 + lane) * 16, 0, 16);
#pragma unroll
    for (int k = 0; k < 2; k++) {
      if (k == 1 && wid != 0) break;
      const int j = wid + 8 * k;
      const u32x4 (&pp)[3] = pq[k];
      LDS_AS char* rb = smem + G2_RES + (j * 16 + 4 * g + q4) * 64 + p4 * 8;
      u16* orow = a.out + (size_t)(mt16 * 128 + ks * 32 + (lane & 15)) * D + nt * 144 + j * 16 + 4 * g;
#pragma unroll
      for (int i = 0; i < 2; i++) {
        const u32x2 hres = __builtin_bit_cast(u32x2, tr_read(rb + i * 32));
        f32x4 v = *(LDS_AS f32x4*)(smem + g2_slot(2) + ((j * 2 + i) * 64 + lane) * 16);
#pragma unroll
        for (int sidx = 0; sidx < 3; sidx++) {
          v[0] += bflo(pp[sidx][2 * i]); v[1] += bfhi(pp[sidx][2 * i]);
          v[2] += bflo(pp[sidx][2 * i + 1]); v[3] += bfhi(pp[sidx][2 * i + 1]);
        }
        const u32 o01 = pk2(v[0], v[1]), o23 = pk2(v[2], v[3]);
        u32x2 ov = {pk2(bflo(hres.x) + bflo(o01), bfhi(hres.x) + bfhi(o01)), pk2(bflo(hres.y) + bflo(o23), bfhi(hres.y) + bfhi(o23))};
        *(u32x2*)(orow + (size_t)i * 16 * D) = ov;
      }
    }
  }
}

extern "C" void kernel_launch(void* const* d_in, const int* in_sizes, int n_in, void* d_out, int out_size, void* d_ws,
                              size_t ws_size, hipStream_t stream) {
  (void)in_sizes; (void)n_in; (void)out_size; (void)ws_size;
  (void)hipFuncSetAttribute((const void*)k2_main, hipFuncAttributeMaxDynamicSharedMemorySize, K2_LDS);
  K1Args a1;
  a1.x = (const u16*)d_in[0]; a1.Are = (const u16*)d_in[1]; a1.Aim = (const u16*)d_in[2];
  a1.Bre = (const u16*)d_in[3]; a1.Bim = (const u16*)d_in[4]; a1.Cre = (const u16*)d_in[5];
  a1.Cim = (const u16*)d_in[6]; a1.ldt = (const u16*)d_in[7]; a1.w1 = (const u16*)d_in[10];
  a1.w3 = (const u16*)d_in[11]; a1.w2 = (const u16*)d_in[12]; a1.ws = (char*)d_ws;
  k1_pre<<<K1_LN + K1_KG + K1_PT, 256, K1_LDS, stream>>>(a1);
  K2Args a2;
  a2.dskip = (const u16*)d_in[8]; a2.w1 = (const u16*)d_in[10]; a2.w3 = (const u16*)d_in[11]; a2.w2 = (const u16*)d_in[12];
  a2.ws = (char*)d_ws; a2.out = (u16*)d_out;
  k2_main<<<256, 512, K2_LDS, stream>>>(a2);
}
```
